```python
import torch

def _softmax(x, axis=-1):
    return torch.softmax(x, dim=axis)
def _gelu(x):
    return torch.nn.functional.gelu(x, approximate="tanh")
def _silu(x):
    return torch.nn.functional.silu(x)
def _sigmoid(x):
    return torch.sigmoid(x)
def _layernorm(x, axis=-1, eps=1e-5):
    m = x.mean(dim=axis, keepdim=True)
    v = x.var(dim=axis, keepdim=True, unbiased=False)
    return (x - m) / torch.sqrt(v + eps)
def _scatter_add(tgt, idx, src, axis=0):
    return tgt.clone().index_add_(axis, idx, src)
def _segment_sum(src, seg, n):
    out = torch.zeros((n,) + src.shape[1:], dtype=src.dtype)
    return out.index_add_(0, seg, src)
def _pad(x, pw):
    flat = tuple(v for pair in reversed(pw) for v in pair)
    return torch.nn.functional.pad(x, flat)

kernel_name = 'recurrent_scan__sliding_block_cached__swiglu_ffn__decode'

def setup_inputs():

    _w = (lambda a: (lambda t: t.to(torch.bfloat16) if t.is_floating_point() else t)(torch.as_tensor(a)))
    def _A_setup():
        B, Sq, D = 8, 1, 512
        Hq, Hkv, Sk, Dh = 32, 8, 4096, 128
        assert Hq % Hkv == 0, (Hq, Hkv)
        return {
            "x": _w((lambda *s: torch.randn(*s, dtype=torch.bfloat16) * 0.05)(B, Sq, D)),
            "k_cache": _w((lambda *s: torch.randn(*s, dtype=torch.bfloat16) * 0.8)(B, Hkv, Sk, Dh)),
            "v_cache": _w((lambda *s: torch.randn(*s, dtype=torch.bfloat16) * 0.2)(B, Hkv, Sk, Dh)),
            "wq": _w((lambda *s: torch.randn(*s, dtype=torch.bfloat16) * 0.8)(D, Hq * Dh) * (D ** -0.5)),
            "wo": _w((lambda *s: torch.randn(*s, dtype=torch.bfloat16) * 0.2)(Hq * Dh, D) * ((Hq * Dh) ** -0.5)),
        }
    _A_in = _A_setup()

    def _B_setup():
        B, S, D, F = 8, 1, 512, 1792
        return {
            "x": _w((lambda *s: torch.randn(*s, dtype=torch.bfloat16) * 0.05)(B, S, D)),
            "w_gate": _w((lambda *s: torch.randn(*s, dtype=torch.bfloat16) * 0.1)(D, F)),
            "w_up": _w((lambda *s: torch.randn(*s, dtype=torch.bfloat16) * 0.1)(D, F)),
            "w_down": _w((lambda *s: torch.randn(*s, dtype=torch.bfloat16) * 0.1)(F, D)),
        }
    _B_in = _B_setup()

    return {
        "A_x": _A_in["x"],
        "A_k_cache": _A_in["k_cache"],
        "A_v_cache": _A_in["v_cache"],
        "A_wq": _A_in["wq"],
        "A_wo": _A_in["wo"],
        "B_x": _B_in["x"],
        "B_w_gate": _B_in["w_gate"],
        "B_w_up": _B_in["w_up"],
        "B_w_down": _B_in["w_down"],
    }

def reference(A_x, A_k_cache, A_v_cache, A_wq, A_wo, B_x, B_w_gate, B_w_up, B_w_down):
    def _A_ref(x, k_cache, v_cache, wq, wo):
        B, Sq, D = x.shape
        Hq, Dh = 32, 128
        Hkv, Sk = k_cache.shape[1], k_cache.shape[2]
        W = min(4096, max(256, Sk // 4))
        rep = Hq // Hkv
        q = (lambda x, *d: x.permute(*d))((x @ wq).reshape(B, Sq, Hq, Dh), 0, 2, 1, 3)
        k = (lambda x, n, axis: torch.repeat_interleave(x, n, dim=axis))(k_cache[:, :, Sk - W:, :], rep, axis=1)
        v = (lambda x, n, axis: torch.repeat_interleave(x, n, dim=axis))(v_cache[:, :, Sk - W:, :], rep, axis=1)
        scores = torch.einsum("bhqd,bhkd->bhqk", q, k) * (Dh ** -0.5)
        p = _softmax(scores, axis=-1)
        o = torch.einsum("bhqk,bhkd->bhqd", p, v)
        return (lambda x, *d: x.permute(*d))(o, 0, 2, 1, 3).reshape(B, Sq, Hq * Dh) @ wo

    def _B_ref(x, w_gate, w_up, w_down):
        g = _silu(x @ w_gate)
        u = x @ w_up
        return (g * u) @ w_down

    x = A_x
    xn = _layernorm(x, axis=-1)
    s = _A_ref(xn, A_k_cache, A_v_cache, A_wq, A_wo)
    g = _silu(_B_ref(xn, B_w_gate, B_w_up, B_w_down))
    return x + s * g
```

```cpp
#include <hip/hip_runtime.h>
#include <hip/hip_bf16.h>
#include <stdint.h>

typedef unsigned short u16;
typedef unsigned v4u __attribute__((ext_vector_type(4)));
__device__ __forceinline__ float bf2f(unsigned b) { return __builtin_bit_cast(float, b << 16); }
__device__ __forceinline__ float rbf(float f) { return __bfloat162float(__float2bfloat16(f)); }
__device__ __forceinline__ u16 f2bf(float f) { __hip_bfloat16 h = __float2bfloat16(f); return __builtin_bit_cast(u16, h); }

constexpr int B = 8, D = 512, HQ = 32, HKV = 8, SK = 4096, DH = 128, W = 1024, F = 1792, NS = 4;

__device__ void layernorm_to_lds(const u16* x, float* xn  ) {
  int tid = threadIdx.x, lane = tid & 63, w = tid >> 6;
  for (int b = w; b < B; b += blockDim.x >> 6) {
    float v[8]; float s = 0.f;
    for (int i = 0; i < 8; ++i) { v[i] = bf2f(x[b * D + lane * 8 + i]); s += v[i]; }
    for (int m = 32; m >= 1; m >>= 1) s += __shfl_xor(s, m);
    float mean = s * (1.f / D);
    float q = 0.f;
    for (int i = 0; i < 8; ++i) { float d = v[i] - mean; q += d * d; }
    for (int m = 32; m >= 1; m >>= 1) q += __shfl_xor(q, m);
    float mb = rbf(mean), vb = rbf(q * (1.f / D));
    float den = rbf(sqrtf(rbf(vb + 1e-5f)));
    for (int i = 0; i < 8; ++i) xn[b * D + lane * 8 + i] = rbf(rbf(v[i] - mb) / den);
  }
  __syncthreads();
}

__global__ void __launch_bounds__(256) k1(const u16* x, const u16* wq, const u16* wg, const u16* wu, u16* q, u16* h) {
  __shared__ float xn[B * D];
  layernorm_to_lds(x, xn);
  int c = blockIdx.x * 64 + (threadIdx.x & 63), part = threadIdx.x >> 6;
  __shared__ float red[2][4][B][64];
  float a[B] = {}, u[B] = {};
  bool isq = c < HQ * DH;
  if (isq) {
    for (int k = part * 128; k < part * 128 + 128; ++k) { float wv = bf2f(wq[k * (HQ * DH) + c]);
      for (int b = 0; b < B; ++b) a[b] += xn[b * D + k] * wv; }
  } else {
    int f = c - HQ * DH;
    for (int k = part * 128; k < part * 128 + 128; ++k) { float g = bf2f(wg[k * F + f]), uu = bf2f(wu[k * F + f]);
      for (int b = 0; b < B; ++b) { a[b] += xn[b * D + k] * g; u[b] += xn[b * D + k] * uu; } }
  }
  for (int b = 0; b < B; ++b) { red[0][part][b][threadIdx.x & 63] = a[b]; red[1][part][b][threadIdx.x & 63] = u[b]; }
  __syncthreads();
  if (part == 0) {
    for (int b = 0; b < B; ++b) {
      int l = threadIdx.x;
      float A = red[0][0][b][l] + red[0][1][b][l] + red[0][2][b][l] + red[0][3][b][l];
      float U = red[1][0][b][l] + red[1][1][b][l] + red[1][2][b][l] + red[1][3][b][l];
      if (isq) q[b * (HQ * DH) + c] = f2bf(A);
      else { float g = rbf(A); g = rbf(g / (1.f + __expf(-g))); h[b * F + (c - HQ * DH)] = f2bf(g * rbf(U)); }
    }
  }
}

__global__ void __launch_bounds__(256) k2(const u16* q, const u16* kc, const u16* vc, float* po, float* pml) {
  int s = blockIdx.x, kvh = blockIdx.y, b = blockIdx.z, tid = threadIdx.x;
  __shared__ float qs[4][DH]; __shared__ float sc[4][256]; __shared__ float redm[4][4], reds[4][4];
  for (int i = tid; i < 512; i += 256) qs[i >> 7][i & 127] = bf2f(q[b * 4096 + kvh * 512 + i]);
  __syncthreads();
  int key = SK - W + s * 256 + tid;
  const v4u* kp = (const v4u*)(kc + ((size_t)(b * HKV + kvh) * SK + key) * DH);
  float a[4] = {};
  for (int c = 0; c < 16; ++c) { v4u kv = kp[c];
    for (int j = 0; j < 4; ++j) { float k0 = bf2f(kv[j] & 0xffff), k1v = bf2f(kv[j] >> 16);
      for (int h = 0; h < 4; ++h) a[h] += qs[h][c * 8 + 2 * j] * k0 + qs[h][c * 8 + 2 * j + 1] * k1v; } }
  float mx[4];
  for (int h = 0; h < 4; ++h) { a[h] = rbf(rbf(a[h]) * 0.08838834764831845f); float m = a[h];
    for (int o = 32; o >= 1; o >>= 1) m = fmaxf(m, __shfl_xor(m, o));
    if ((tid & 63) == 0) redm[h][tid >> 6] = m; }
  __syncthreads();
  for (int h = 0; h < 4; ++h) { mx[h] = fmaxf(fmaxf(redm[h][0], redm[h][1]), fmaxf(redm[h][2], redm[h][3]));
    float p = __expf(a[h] - mx[h]); sc[h][tid] = p; float t = p;
    for (int o = 32; o >= 1; o >>= 1) t += __shfl_xor(t, o);
    if ((tid & 63) == 0) reds[h][tid >> 6] = t; }
  __syncthreads();
  int d = tid & 127, half = tid >> 7;
  float o[4] = {};
  const u16* vp = vc + ((size_t)(b * HKV + kvh) * SK + SK - W + s * 256 + half * 128) * DH + d;
  for (int k = 0; k < 128; ++k) { float v = bf2f(vp[k * DH]);
    for (int h = 0; h < 4; ++h) o[h] += sc[h][half * 128 + k] * v; }
  __shared__ float ored[4][128];
  if (half == 1) for (int h = 0; h < 4; ++h) ored[h][d] = o[h];
  __syncthreads();
  if (half == 0) for (int h = 0; h < 4; ++h) po[(((b * HQ) + kvh * 4 + h) * NS + s) * DH + d] = o[h] + ored[h][d];
  if (tid < 4) { int h = tid; pml[(((b * HQ) + kvh * 4 + h) * NS + s) * 2] = mx[h];
    pml[(((b * HQ) + kvh * 4 + h) * NS + s) * 2 + 1] = reds[h][0] + reds[h][1] + reds[h][2] + reds[h][3]; }
}

__global__ void __launch_bounds__(512) k3(const float* po, const float* pml, const u16* h, const u16* wo, const u16* wd, float* ps) {
  __shared__ float o[B][128];
  int blk = blockIdx.x, tid = threadIdx.x;
  const u16* w;
  if (blk < 32) {
    for (int i = tid; i < B * 128; i += 512) { int b = i >> 7, d = i & 127; int base = (b * HQ + blk) * NS;
      float M = -INFINITY; for (int s = 0; s < NS; ++s) M = fmaxf(M, pml[(base + s) * 2]);
      float L = 0.f, acc = 0.f;
      for (int s = 0; s < NS; ++s) { float wt = __expf(pml[(base + s) * 2] - M); L += wt * pml[(base + s) * 2 + 1]; acc += wt * po[(base + s) * DH + d]; }
      o[b][d] = rbf(acc / L); }
    w = wo + (size_t)blk * 128 * D;
  } else {
    for (int i = tid; i < B * 128; i += 512) { int b = i >> 7, d = i & 127; o[b][d] = bf2f(h[b * F + (blk - 32) * 128 + d]); }
    w = wd + (size_t)(blk - 32) * 128 * D;
  }
  __syncthreads();
  float a[B] = {};
  for (int k = 0; k < 128; ++k) { float wv = bf2f(w[k * D + tid]); for (int b = 0; b < B; ++b) a[b] += o[b][k] * wv; }
  for (int b = 0; b < B; ++b) ps[(blk * B + b) * D + tid] = a[b];
}

__global__ void __launch_bounds__(512) k4(const u16* x, const float* ps, u16* out) {
  int b = blockIdx.x, n = threadIdx.x;
  float s = 0.f, f = 0.f;
  for (int i = 0; i < 32; ++i) s += ps[(i * B + b) * D + n];
  for (int i = 32; i < 46; ++i) f += ps[(i * B + b) * D + n];
  s = rbf(s); f = rbf(f);
  float g = rbf(f / (1.f + __expf(-f)));
  out[b * D + n] = f2bf(bf2f(x[b * D + n]) + rbf(s * g));
}

extern "C" void kernel_launch(void* const* d_in, const int* in_sizes, int n_in, void* d_out, int out_size, void* d_ws, size_t ws_size, hipStream_t stream) {
  const u16* x = (const u16*)d_in[0]; const u16* kc = (const u16*)d_in[1]; const u16* vc = (const u16*)d_in[2];
  const u16* wq = (const u16*)d_in[3]; const u16* wo = (const u16*)d_in[4];
  const u16* wg = (const u16*)d_in[6]; const u16* wu = (const u16*)d_in[7]; const u16* wd = (const u16*)d_in[8];
  char* ws = (char*)d_ws;
  u16* q = (u16*)ws; u16* h = (u16*)(ws + 65536); float* po = (float*)(ws + 131072); float* pml = (float*)(ws + 131072 + 524288);
  float* ps = (float*)(ws + 1048576);
  k1<<<(4096 + 1792) / 64, 256, 0, stream>>>(x, wq, wg, wu, q, h);
  k2<<<dim3(NS, HKV, B), 256, 0, stream>>>(q, kc, vc, po, pml);
  k3<<<46, 512, 0, stream>>>(po, pml, h, wo, wd, ps);
  k4<<<8, 512, 0, stream>>>(x, ps, (u16*)d_out);
}
```

```cpp
#include <hip/hip_runtime.h>

typedef unsigned short u16;
typedef unsigned u32;
typedef unsigned long long u64;
typedef u32 v4u __attribute__((ext_vector_type(4)));
typedef u32 v2u __attribute__((ext_vector_type(2)));
typedef float f32x4 __attribute__((ext_vector_type(4)));
typedef short s16x4 __attribute__((ext_vector_type(4)));
typedef __bf16 bf16x8 __attribute__((ext_vector_type(8)));
typedef __bf16 bf16x2 __attribute__((ext_vector_type(2)));
#define LAS __attribute__((address_space(3)))
#define GAS __attribute__((address_space(1)))
typedef __amdgpu_buffer_rsrc_t rsrc_t;

namespace {
constexpr int kD = 512, kSK = 4096, kW0 = 3072, kF = 1792;
constexpr int RK = 0, RV = 65536, RWQ = RV + 49152, RA = 131072, RD = 147456, RM = 155648;
constexpr int XN = RM, OL = RM, FR = RM + 1024, QL = RM + 2048, PL = RM + 3328, MLM = RM + 5376, MLL = RM + 5504,
              HL = RM + 5632, FL = RM + 6144;
constexpr int F_LP = FL, F_XN = FL + 4, F_WQ = FL + 8, F_GU = FL + 12, F_Q = FL + 16, F_D = FL + 20, F_P = FL + 24, F_H = FL + 28;
constexpr int EL = FL + 64, ST = FL + 320, RX = RD;
constexpr int LDS_BYTES = 163840;
constexpr int WS_E = 0, WS_QP = 4096, WS_HP = WS_QP + 4 * 8 * 2048 * 8, WS_OP = WS_HP + 4 * 2 * 8 * 896 * 8,
              WS_ML = WS_OP + 8 * 32 * 4 * 64 * 8, WS_SP = WS_ML + 8 * 32 * 4 * 16, WS_DP = WS_SP + 64 * 8 * 512 * 8,
              WS_END = WS_DP + 56 * 8 * 512 * 8;
constexpr int OOB = 0x7ffffff0;
constexpr int AUX_NT = 2, AUX_SC1 = 16, RSRC3 = 0x00020000;
constexpr u32 kSpinLds = 1u << 22, kSpinGlb = 1u << 16;

__device__ __forceinline__ float bflo(u32 w) { return __builtin_bit_cast(float, w << 16); }
__device__ __forceinline__ float bfhi(u32 w) { return __builtin_bit_cast(float, w & 0xffff0000u); }
__device__ __forceinline__ u32 pk(float lo, float hi) {
  bf16x2 v = {(__bf16)lo, (__bf16)hi};
  return __builtin_bit_cast(u32, v);
}
__device__ __forceinline__ float rbf(float f) { return (float)(__bf16)f; }
__device__ __forceinline__ f32x4 mfma(v4u a, v4u b, f32x4 c) {
  return __builtin_amdgcn_mfma_f32_16x16x32_bf16(__builtin_bit_cast(bf16x8, a), __builtin_bit_cast(bf16x8, b), c, 0, 0, 0);
}
template <int CTRL> __device__ __forceinline__ float dpp(float x) {
  return __builtin_bit_cast(float, __builtin_amdgcn_mov_dpp(__builtin_bit_cast(int, x), CTRL, 0xf, 0xf, true));
}
constexpr int XOR1 = 0xB1, XOR2 = 0x4E, XOR7 = 0x141, XOR8 = 0x128, ROR4 = 0x124;
__device__ __forceinline__ float x16_sum(float x) {
  auto s = __builtin_amdgcn_permlane16_swap(__float_as_uint(x), __float_as_uint(x), false, false);
  return __uint_as_float(s[0]) + __uint_as_float(s[1]);
}
__device__ __forceinline__ float x32_sum(float x) {
  auto s = __builtin_amdgcn_permlane32_swap(__float_as_uint(x), __float_as_uint(x), false, false);
  return __uint_as_float(s[0]) + __uint_as_float(s[1]);
}
__device__ __forceinline__ float x16_max(float x) {
  auto s = __builtin_amdgcn_permlane16_swap(__float_as_uint(x), __float_as_uint(x), false, false);
  return fmaxf(__uint_as_float(s[0]), __uint_as_float(s[1]));
}
__device__ __forceinline__ float x32_max(float x) {
  auto s = __builtin_amdgcn_permlane32_swap(__float_as_uint(x), __float_as_uint(x), false, false);
  return fmaxf(__uint_as_float(s[0]), __uint_as_float(s[1]));
}
__device__ __forceinline__ float wave_sum(float x) {
  x += dpp<XOR1>(x); x += dpp<XOR2>(x); x += dpp<XOR7>(x); x += dpp<XOR8>(x);
  return x32_sum(x16_sum(x));
}
__device__ __forceinline__ float silu(float x) { return x * __builtin_amdgcn_rcpf(1.f + __builtin_amdgcn_exp2f(x * -1.4426950408889634f)); }

__device__ __forceinline__ void lds_set(int addr, u32 val) {
  asm volatile("ds_write_b32 %0, %1" ::"v"(addr), "v"(val) : "memory");
}
__device__ __forceinline__ u32 lds_get(int addr) {
  u32 r;
  asm volatile("ds_read_b32 %0, %1\n\ts_waitcnt lgkmcnt(0)" : "=v"(r) : "v"(addr) : "memory");
  return __builtin_amdgcn_readfirstlane(r);
}
__device__ __forceinline__ u32 lds_wait_ge(int addr, u32 want) {
  u32 v = lds_get(addr);
  for (u32 n = 0; v < want && n < kSpinLds; ++n) { __builtin_amdgcn_s_sleep(1); v = lds_get(addr); }
  return v;
}
__device__ __forceinline__ u32 lds_spin_ge(int addr, u32 want) {
  u32 v = lds_get(addr);
  for (u32 n = 0; v < want && n < kSpinLds; ++n) v = lds_get(addr);
  return v;
}
__device__ __forceinline__ void lds_arrive(int addr, int) {
  asm volatile("s_mov_b64 exec, 1\n\tds_add_u32 %0, %1\n\ts_mov_b64 exec, -1" ::"v"(addr), "v"(1u) : "memory");
}
__device__ __forceinline__ v4u ws_ld(rsrc_t rs, int off) { return __builtin_amdgcn_raw_buffer_load_b128(rs, off, 0, AUX_SC1); }
__device__ __forceinline__ void ws_st(rsrc_t rs, int off, v4u v) { __builtin_amdgcn_raw_buffer_store_b128(v, rs, off, 0, AUX_SC1); }
__device__ __forceinline__ bool tag_ok(v4u u, u32 tag) { return u.y == tag && u.w == tag; }
__device__ __forceinline__ bool all_of(bool ok) { return __builtin_amdgcn_ballot_w64(!ok) == 0ull; }
typedef int s4i __attribute__((ext_vector_type(4)));
#define POLL_REGS "v96", "v97", "v98", "v99", "v100", "v101", "v102", "v103"
__device__ __forceinline__ void poll_a(s4i rs, int off) {
  asm volatile("buffer_load_dwordx4 v[96:99], %0, %1, 0 offen sc1" ::"v"(off), "s"(rs) : "memory", POLL_REGS);
}
__device__ __forceinline__ void poll_b(s4i rs, int off) {
  asm volatile("buffer_load_dwordx4 v[100:103], %0, %1, 0 offen sc1" ::"v"(off), "s"(rs) : "memory", POLL_REGS);
}
__device__ __forceinline__ v2u poll_end(s4i rs, int off, u32 tag, u32& left) {
  u32 x, z, cnt;
  int ad;
  u64 ok, t;
#define POLL_STEP(r0, r1, r2, r3)                                                                   \
  "s_waitcnt vmcnt(1)\n\t"                                                                          \
  "v_cmp_eq_u32_e32 vcc, %[tag], v" #r1 "\n\t"                                                      \
  "v_cmp_eq_u32_e64 %[t], %[tag], v" #r3 "\n\t"                                                     \
  "s_and_b64 vcc, vcc, %[t]\n\t"                                                                    \
  "s_andn2_b64 vcc, vcc, %[ok]\n\t"                                                                 \
  "v_cndmask_b32_e32 %[x], %[x], v" #r0 ", vcc\n\t"                                                 \
  "v_cndmask_b32_e32 %[z], %[z], v" #r2 ", vcc\n\t"                                                 \
  "s_or_b64 %[ok], %[ok], vcc\n\t"                                                                  \
  "s_cmp_eq_u64 %[ok], -1\n\t"                                                                      \
  "s_cbranch_scc1 .Lpoll_done_%=\n\t"                                                               \
  "v_cndmask_b32_e64 %[ad], %[off], %[oob], %[ok]\n\t"                                              \
  "buffer_load_dwordx4 v[" #r0 ":" #r3 "], %[ad], %[rs], 0 offen sc1\n\t"
  asm volatile("s_mov_b64 %[ok], 0\n\t"
               "s_movk_i32 %[cnt], 0x1000\n"
               ".Lpoll_loop_%=:\n\t" POLL_STEP(96, 97, 98, 99) POLL_STEP(100, 101, 102, 103)
               "s_sub_u32 %[cnt], %[cnt], 1\n\t"
               "s_cmp_lg_u32 %[cnt], 0\n\t"
               "s_cbranch_scc1 .Lpoll_loop_%=\n"
               ".Lpoll_done_%=:\n\t"
               "s_nop 0"
               : [x] "=&v"(x), [z] "=&v"(z), [ad] "=&v"(ad), [ok] "=&s"(ok), [t] "=&s"(t), [cnt] "=&s"(cnt)
               : [off] "v"(off), [oob] "v"(OOB), [rs] "s"(rs), [tag] "s"(tag)
               : "memory", "vcc", "scc", POLL_REGS);
  left = cnt;
  return v2u{x, z};
}
__device__ __forceinline__ v2u ws_take(s4i rs, int off, v4u u, u32 tag, u32& n) {
  n = 0x1001;
  if (all_of(tag_ok(u, tag))) return v2u{u.x, u.z};
  poll_a(rs, off);
  __builtin_amdgcn_s_sleep(4);
  poll_b(rs, off);
  return poll_end(rs, off, tag, n);
}
__device__ __forceinline__ u32 now() { return (u32)__builtin_amdgcn_s_memrealtime(); }
__device__ __forceinline__ void wait_until(u32 t0, u32 dt) {
  while (now() - t0 < dt) __builtin_amdgcn_s_sleep(1);
}
__device__ __forceinline__ v4u tr_frag(LAS unsigned char* p, int half) {
  const v2u a = __builtin_bit_cast(v2u, __builtin_amdgcn_ds_read_tr16_b64_v4i16((LAS s16x4*)p));
  const v2u b = __builtin_bit_cast(v2u, __builtin_amdgcn_ds_read_tr16_b64_v4i16((LAS s16x4*)(p + half)));
  return v4u{a.x, a.y, b.x, b.y};
}
}

#define LAG 5
#define LAGK 5
#define SPDELAY 8
#define TQ1 310
#define TQ2 420
#define PGAP 3
#define QPOLL 6
#define HPOLL 9
#define STAMP(i)
#define STAMPV(i, v)
#define STAMP_DUMP()

__global__ void __launch_bounds__(576, 3)
fused_decode_block(const u16* __restrict__ x, const u16* __restrict__ kc, const u16* __restrict__ vc, const u16* __restrict__ wq,
                   const u16* __restrict__ wo, const u16* __restrict__ wg, const u16* __restrict__ wu, const u16* __restrict__ wd,
                   u16* __restrict__ out, unsigned char* __restrict__ ws) {
  __shared__ __attribute__((aligned(16))) unsigned char lds[LDS_BYTES];
  LAS unsigned char* L = (LAS unsigned char*)lds;
  const int tid = threadIdx.x, lane = tid & 63, wave = __builtin_amdgcn_readfirstlane(tid >> 6);
  asm volatile("" ::"s"(x), "s"(kc), "s"(vc), "s"(wq), "s"(wo), "s"(wg), "s"(wu), "s"(wd), "s"(out), "s"(ws));
  const u32 t0 = now();
  const int c = blockIdx.x, g8 = c & 7, r = c >> 3;
  const int fb = c >> 5, fc = (c & 31) * 16;
  const int j = c >> 6, t64 = c & 63;
  const bool has_gu = t64 < 56;
  const int fg = (r >> 2) * 8 + g8, cg = r & 3;
  const bool has_dn = fg < 56;
  const int dt = r >> 2;
  const int ab = r >> 2, as = r & 3;
  const rsrc_t rw = __builtin_amdgcn_make_buffer_rsrc((void*)ws, 0, WS_END, RSRC3);
  const s4i rp = {(int)(u32)(u64)ws, (int)((u32)((u64)ws >> 32) & 0xffffu), WS_END, RSRC3};
  const u32 TH_K = has_gu ? 5 : 3, TH_V = TH_K + 8, TH_DN = TH_V + 9, TH_WO = TH_V + 8 + (has_dn ? 1 : 0) + 2;
  STAMP(0);

  if (__builtin_expect(wave == 0, 1)) {
    const int r8 = lane >> 3, r4 = lane >> 4;
    const int sw128 = ((lane & 7) ^ ((r4 & 3) << 1)) * 16;
    const int swV = ((lane & 15) ^ (r4 << 1)) * 16;
    const int swK = ((lane & 15) ^ r4) * 16;
    u32 cnt, lpv = 1;
    __builtin_amdgcn_s_setprio(3);
#define GLDS(rs, ldsoff, voff, soff) __builtin_amdgcn_raw_ptr_buffer_load_lds(rs, (LAS void*)(L + (ldsoff)), 16, voff, soff, 0, AUX_NT)
#define GROUP_DONE_L(LG) do { asm volatile("s_waitcnt vmcnt(%0)" ::"n"((LG) * 8) : "memory"); ++cnt; if (cnt + 1 > lpv + (LG)) { lpv = cnt + 1 - (LG); lds_set(F_LP, lpv); } } while (0)
#define GROUP_DONE() GROUP_DONE_L(LAG)
    {
      const rsrc_t rs = __builtin_amdgcn_make_buffer_rsrc((void*)x, 0, 8192, RSRC3);
#pragma unroll
      for (int i = 0; i < 8; ++i) __builtin_amdgcn_raw_ptr_buffer_load_lds(rs, (LAS void*)(L + RX + i * 1024), 16, lane * 16, i * 1024, 0, 0);
      __builtin_amdgcn_raw_ptr_buffer_load_lds(rw, (LAS void*)(L + EL), 4, lane * 4, WS_E, 0, AUX_SC1);
    }
    {
      const rsrc_t rs = __builtin_amdgcn_make_buffer_rsrc((void*)wq, 0, 512 * 4096 * 2, RSRC3);
      const int vo = r8 * 8192 + sw128, so = j * 128 * 8192 + t64 * 128;
#pragma unroll
      for (int i = 0; i < 16; ++i) GLDS(rs, RWQ + i * 1024, vo, so + i * 8 * 8192);
    }
    STAMP(1);
    if (has_gu) {
      const rsrc_t rs = __builtin_amdgcn_make_buffer_rsrc((void*)(t64 < 28 ? wg : wu), 0, 512 * kF * 2, RSRC3);
      const int vo = r8 * (kF * 2) + sw128, so = j * 128 * (kF * 2) + (t64 < 28 ? t64 : t64 - 28) * 128;
#pragma unroll
      for (int i = 0; i < 16; ++i) GLDS(rs, RA + i * 1024, vo, so + i * 8 * (kF * 2));
      __builtin_amdgcn_s_barrier();
      asm volatile("s_waitcnt vmcnt(32)" ::: "memory");
      cnt = 4;
    } else {
      __builtin_amdgcn_s_barrier();
      asm volatile("s_waitcnt vmcnt(16)" ::: "memory");
      cnt = 2;
    }
    lds_set(F_LP, 1u);
    u32 xfin;
    asm volatile("ds_read_u16 %0, %1" : "=v"(xfin) : "v"(RX + (fb * kD + fc + (lane & 15)) * 2) : "memory");
    const u32 tag = lds_get(EL) + 1u;
    asm volatile("" : "+v"(xfin));
    STAMP(2);
    const size_t kvoff = ((size_t)(ab * 8 + g8) * kSK + kW0 + as * 256) * 128;
    {
      const rsrc_t rs = __builtin_amdgcn_make_buffer_rsrc((void*)(kc + kvoff), 0, 65536, RSRC3);
      const int vo = r4 * 256;
#pragma unroll 1
      for (int n = 0; n < 8; ++n) {
#pragma unroll
        for (int i = 0; i < 8; ++i) GLDS(rs, RK + n * 8192 + i * 1024, vo + (swK ^ ((i & 3) * 64)), n * 8192 + i * 1024);
        GROUP_DONE_L(LAGK);
      }
    }
    STAMP(3);
    {
      const rsrc_t rs = __builtin_amdgcn_make_buffer_rsrc((void*)(vc + kvoff), 0, 65536, RSRC3);
      const int vo = r4 * 256;
#pragma unroll 1
      for (int n = 0; n < 8; ++n) {
        if (n == 6) lds_wait_ge(F_WQ, 4);
#pragma unroll
        for (int i = 0; i < 8; ++i) GLDS(rs, RV + n * 8192 + i * 1024, vo + (swV ^ ((i & 1) * 128)), n * 8192 + i * 1024);
        GROUP_DONE();
      }
    }
    STAMP(4);
    if (has_dn) {
      const rsrc_t rs = __builtin_amdgcn_make_buffer_rsrc((void*)wd, 0, kF * 512 * 2, RSRC3);
      const int vo = r4 * 1024, so = fg * 32 * 1024 + cg * 256;
#pragma unroll
      for (int i = 0; i < 8; ++i) GLDS(rs, RD + i * 1024, vo + (swV ^ ((i & 1) * 128)), so + i * 4096);
      GROUP_DONE();
    }
    if (has_gu) lds_wait_ge(F_GU, 4);
    {
      const rsrc_t rs = __builtin_amdgcn_make_buffer_rsrc((void*)wo, 0, 4096 * 512 * 2, RSRC3);
      const int vo = r4 * 1024, so = (g8 * 512 + dt * 16) * 1024 + cg * 256;
#pragma unroll
      for (int n = 0; n < 2; ++n) {
#pragma unroll
        for (int i = 0; i < 8; ++i)
          GLDS(rs, RA + n * 8192 + i * 1024, vo + (swV ^ ((i & 1) * 128)), so + ((2 * n + (i >> 2)) * 128 + (i & 3) * 4) * 1024);
        GROUP_DONE();
      }
    }
    STAMP(5);
    asm volatile("s_waitcnt vmcnt(32)" ::: "memory"); lds_set(F_LP, cnt - 3);
    asm volatile("s_waitcnt vmcnt(24)" ::: "memory"); lds_set(F_LP, cnt - 2);
    STAMP(6);
    asm volatile("s_waitcnt vmcnt(16)" ::: "memory"); lds_set(F_LP, cnt - 1);
    asm volatile("s_waitcnt vmcnt(8)" ::: "memory");  lds_set(F_LP, cnt);
    asm volatile("s_waitcnt vmcnt(0)" ::: "memory");  lds_set(F_LP, cnt + 1);
    STAMP(7);
    __builtin_amdgcn_s_barrier();
    lds_spin_ge(F_D, 7);
    LAS f32x4* fr = (LAS f32x4*)(L + FR + (lane & 15) * 32);
    const f32x4 b0 = fr[32], b1 = fr[33];
    const float gate = rbf(silu(rbf(((b0[0] + b0[1]) + (b0[2] + b0[3])) + ((b1[0] + b1[1]) + b1[2]))));
    asm volatile("" ::"v"(gate));
    __builtin_amdgcn_s_barrier();
    asm volatile("" ::: "memory");
    const f32x4 a0 = fr[0], a1 = fr[1];
    const float sv = rbf(((a0[0] + a0[1]) + (a0[2] + a0[3])) + ((a1[0] + a1[1]) + (a1[2] + a1[3])));
    const float o = bflo(xfin) + rbf(sv * gate);
    if (lane < 16) out[fb * kD + fc + lane] = (u16)pk(o, o);
    if (c == 0 && lane == 0) __builtin_amdgcn_raw_buffer_store_b32(tag, rw, WS_E, 0, AUX_SC1);
    STAMP(8);
    STAMP_DUMP();
    return;
  }

  const int cw = wave - 1, lc = lane & 15, lg = lane >> 4;
  if (cw == 0 && lane < 8) lds_set(FL + lane * 4, 0u);
  asm volatile("s_waitcnt lgkmcnt(0)" ::: "memory");
  __builtin_amdgcn_s_barrier();
  u32 lp = lds_spin_ge(F_LP, 1);
  STAMP(1);
  const u32 tag = lds_get(EL) + 1u;
  {
    const v4u xr = *(LAS v4u*)(L + RX + cw * 1024 + lane * 16);
    float v[8];
#pragma unroll
    for (int i = 0; i < 4; ++i) { v[2 * i] = bflo(xr[i]); v[2 * i + 1] = bfhi(xr[i]); }
    const float x0 = __builtin_bit_cast(float, __builtin_amdgcn_readfirstlane(xr[0] << 16));
    float s0 = 0.f, q0 = 0.f;
#pragma unroll
    for (int i = 0; i < 8; ++i) { const float d = v[i] - x0; s0 += d; q0 = fmaf(d, d, q0); }
    s0 = wave_sum(s0) * (1.f / kD);
    q0 = wave_sum(q0) * (1.f / kD);
    const float mb = rbf(x0 + s0), vb = rbf(fmaxf(fmaf(-s0, s0, q0), 0.f));
    const float rden = __builtin_amdgcn_rcpf(rbf(__builtin_amdgcn_sqrtf(rbf(vb + 1e-5f))));
#pragma unroll
    for (int i = 0; i < 8; ++i) v[i] = rbf(v[i] - mb) * rden;
    if (lg == j) {
      const int ks = lc >> 2, o = lc & 3, gq = (o & 1) * 2, hf = o >> 1;
      LAS v2u* p = (LAS v2u*)(L + XN + ((ks * 4 + gq) * 8 + cw) * 16 + hf * 8);
      p[0] = v2u{pk(v[0], v[1]), pk(v[2], v[3])};
      p[16] = v2u{pk(v[4], v[5]), pk(v[6], v[7])};
    }
    lds_arrive(F_XN, lane);
  }
  STAMP(2);
  const int tq = lc >> 2, tp = lc & 3, row0 = 4 * lg + tq;
  if (cw < 4 || has_gu) {
    const int nt = cw & 3;
    lds_spin_ge(F_XN, 8);
    v4u xf[4];
#pragma unroll
    for (int ks = 0; ks < 4; ++ks) xf[ks] = *(LAS v4u*)(L + XN + ((ks * 4 + lg) * 8 + (lc & 7)) * 16);
    lp = lds_spin_ge(F_LP, cw < 4 ? 3u : 5u);
    STAMP(3);
    LAS unsigned char* base = L + (cw < 4 ? RWQ : RA) + row0 * 128 + (((2 * nt + (tp >> 1)) ^ (((row0 >> 1) & 3) << 1)) * 16) + (tp & 1) * 8;
    f32x4 acc = {0.f, 0.f, 0.f, 0.f};
#pragma unroll
    for (int ks = 0; ks < 4; ++ks) acc = mfma(tr_frag(base + ks * 4096, 2048), xf[ks], acc);
    if (lc < 8) {
      const v4u o = {pk(acc[0], acc[1]), tag, pk(acc[2], acc[3]), tag};
      const int off = cw < 4 ? WS_QP + ((j * 8 + lc) * 2048 + t64 * 32 + nt * 8 + lg * 2) * 8
                             : WS_HP + (((j * 2 + (t64 >= 28)) * 8 + lc) * 896 + (t64 >= 28 ? t64 - 28 : t64) * 32 + nt * 8 + lg * 2) * 8;
      ws_st(rw, off, o);
    }
    lds_arrive(cw < 4 ? F_WQ : F_GU, lane);
    STAMP(4);
  }
  const int hoff = has_dn ? WS_HP + (((((lane >> 3) & 3) * 2 + (lane >> 5)) * 8 + cw) * 896 + fg * 16 + (lane & 7) * 2) * 8 : OOB;
  v4u uh;
  u32 n;
  {
    const int qoff = WS_QP + ((lg * 8 + ab) * 2048 + g8 * 256 + (cw * 16 + lc) * 2) * 8;
    wait_until(t0, TQ1);
    STAMP(5);
    const v4u uq4 = ws_ld(rw, qoff);
    __builtin_amdgcn_sched_barrier(0);
    wait_until(t0, TQ2);
    uh = ws_ld(rw, hoff);
    __builtin_amdgcn_sched_barrier(0);
    const v2u uq = ws_take(rp, qoff, uq4, tag, n);
    STAMPV(20, 0x1001 - n);
    STAMP(6);
    const float q0 = x32_sum(x16_sum(bflo(uq.x))), q1 = x32_sum(x16_sum(bfhi(uq.x))), q2 = x32_sum(x16_sum(bflo(uq.y))),
                q3 = x32_sum(x16_sum(bfhi(uq.y)));
    if (lg == 0) {
      const int d = cw * 64 + lc * 4;
      *(LAS v2u*)(L + QL + (d >> 7) * 272 + (d & 127) * 2) = v2u{pk(q0, q1), pk(q2, q3)};
    }
    lds_arrive(F_Q, lane);
  }
  {
    if (lp < TH_K + cw + 1) lp = lds_wait_ge(F_LP, TH_K + cw + 1);
    v4u kf[8];
#pragma unroll
    for (int i = 0; i < 8; ++i) kf[i] = *(LAS v4u*)(L + RK + (cw * 32 + (i >> 2) * 16 + lc) * 256 + ((((i & 3) * 4 + lg) ^ lc) * 16));
    lds_spin_ge(F_Q, 8);
    STAMP(7);
    v4u qf[4];
#pragma unroll
    for (int ks = 0; ks < 4; ++ks) qf[ks] = *(LAS v4u*)(L + QL + (lc & 3) * 272 + ks * 64 + lg * 16);
    f32x4 a0 = {0.f, 0.f, 0.f, 0.f}, a1 = {0.f, 0.f, 0.f, 0.f};
#pragma unroll
    for (int ks = 0; ks < 4; ++ks) { a0 = mfma(kf[ks], qf[ks], a0); a1 = mfma(kf[4 + ks], qf[ks], a1); }
    float sc[8];
#pragma unroll
    for (int i = 0; i < 4; ++i) { sc[i] = rbf(rbf(a0[i]) * 0.08838834764831845f); sc[4 + i] = rbf(rbf(a1[i]) * 0.08838834764831845f); }
    float m = fmaxf(fmaxf(fmaxf(sc[0], sc[1]), fmaxf(sc[2], sc[3])), fmaxf(fmaxf(sc[4], sc[5]), fmaxf(sc[6], sc[7])));
    m = x32_max(x16_max(m));
#pragma unroll
    for (int i = 0; i < 8; ++i) sc[i] = __builtin_amdgcn_exp2f((sc[i] - m) * 1.4426950408889634f);
    if (lc < 4) *(LAS v4u*)(L + PL + ((cw * 4 + lg) * 4 + lc) * 16) = v4u{pk(sc[0], sc[1]), pk(sc[2], sc[3]), pk(sc[4], sc[5]), pk(sc[6], sc[7])};
    const float l = x32_sum(x16_sum(((sc[0] + sc[1]) + (sc[2] + sc[3])) + ((sc[4] + sc[5]) + (sc[6] + sc[7]))));
    if (lane < 4) { *(LAS float*)(L + MLM + (lane * 8 + cw) * 4) = m; *(LAS float*)(L + MLL + (lane * 8 + cw) * 4) = l; }
    lds_arrive(F_P, lane);
  }
  lds_spin_ge(F_P, 8);
  STAMP(8);
  const int wb = row0 * 256 + (((2 * cw + (tp >> 1)) ^ ((row0 & 7) << 1)) * 16) + (tp & 1) * 8;
  {
    const f32x4 m0 = *(LAS f32x4*)(L + MLM + (lc & 3) * 32), m1 = *(LAS f32x4*)(L + MLM + (lc & 3) * 32 + 16);
    v4u pf[8];
#pragma unroll
    for (int i = 0; i < 8; ++i) pf[i] = *(LAS v4u*)(L + PL + ((i * 4 + lg) * 4 + (lc & 3)) * 16);
    const float M = fmaxf(fmaxf(fmaxf(m0[0], m0[1]), fmaxf(m0[2], m0[3])), fmaxf(fmaxf(m1[0], m1[1]), fmaxf(m1[2], m1[3])));
    float f[8];
#pragma unroll
    for (int i = 0; i < 4; ++i) {
      f[i] = __builtin_amdgcn_exp2f((m0[i] - M) * 1.4426950408889634f);
      f[4 + i] = __builtin_amdgcn_exp2f((m1[i] - M) * 1.4426950408889634f);
    }
    if (cw == 0 && lane < 4) {
      const f32x4 l0 = *(LAS f32x4*)(L + MLL + lane * 32), l1 = *(LAS f32x4*)(L + MLL + lane * 32 + 16);
      const float l = (fmaf(l0[0], f[0], l0[1] * f[1]) + fmaf(l0[2], f[2], l0[3] * f[3])) + (fmaf(l1[0], f[4], l1[1] * f[5]) + fmaf(l1[2], f[6], l1[3] * f[7]));
      ws_st(rw, WS_ML + (((ab * 8 + g8) * 4 + as) * 4 + lane) * 16, v4u{__float_as_uint(M), tag, __float_as_uint(l), tag});
    }
    const f32x4 z = {0.f, 0.f, 0.f, 0.f};
    f32x4 acc = z;
    if (lp < TH_V + 5) lp = lds_wait_ge(F_LP, TH_V + 5);
#pragma unroll
    for (int i = 0; i < 5; ++i) acc += f[i] * mfma(tr_frag(L + RV + wb + i * 8192, 4096), pf[i], z);
#pragma unroll
    for (int i = 5; i < 8; ++i) {
      if (lp < TH_V + i + 1) lp = lds_spin_ge(F_LP, TH_V + i + 1);
      acc += f[i] * mfma(tr_frag(L + RV + wb + i * 8192, 4096), pf[i], z);
    }
    if (lc < 4)
      ws_st(rw, WS_OP + ((((g8 * 8 + cw) * 8 + ab) * 4 + as) * 16 + lc * 4 + lg) * 16, v4u{pk(acc[0], acc[1]), tag, pk(acc[2], acc[3]), tag});
    STAMP(9);
  }
  const int pcol = cg * 128 + cw * 16 + lg * 4 + (lc >> 3) * 2;
  const int ooff = WS_OP + ((((g8 * 8 + dt) * 8 + cw) * 4 + lg) * 16 + lc) * 16;
  const int moff = WS_ML + (((cw * 8 + g8) * 4 + lg) * 4 + (lc >> 2)) * 16;
  const int fp = cw * 8 + (lane >> 3), fpr = lane & 7;
  const int soff = WS_SP + ((fp * 8 + fb) * 512 + fc + fpr * 2) * 8, doff = cw < 7 ? WS_DP + ((fp * 8 + fb) * 512 + fc + fpr * 2) * 8 : OOB;
  const v4u um4 = ws_ld(rw, moff);
  __builtin_amdgcn_sched_barrier(0);
  {
    v2u uh2 = {0u, 0u};
    if (has_dn) uh2 = ws_take(rp, hoff, uh, tag, n);
    STAMPV(21, 0x1001 - n);
    float hv[4] = {bflo(uh2.x), bfhi(uh2.x), bflo(uh2.y), bfhi(uh2.y)};
#pragma unroll
    for (int i = 0; i < 4; ++i) {
      float v = hv[i];
      v += dpp<XOR8>(v);
      v = rbf(x16_sum(v));
      auto sw = __builtin_amdgcn_permlane32_swap(__float_as_uint(v), __float_as_uint(v), false, false);
      hv[i] = rbf(silu(__uint_as_float(sw[0]))) * __uint_as_float(sw[1]);
    }
    if (lane < 8)
      *(LAS v2u*)(L + HL + ((lane & 3) * 8 + cw) * 16 + (lane >> 2) * 8) = v2u{pk(hv[0], hv[1]), pk(hv[2], hv[3])};
    lds_arrive(F_H, lane);
    if (lp < TH_V + 9) lp = lds_spin_ge(F_LP, TH_V + 9);
    const v4u df = tr_frag(L + RD + wb, 4096);
    lds_spin_ge(F_H, 8);
    const f32x4 acc = mfma(df, *(LAS v4u*)(L + HL + (lg * 8 + (lc & 7)) * 16), f32x4{0.f, 0.f, 0.f, 0.f});
    ws_st(rw, has_dn ? WS_DP + ((fg * 8 + (lc & 7)) * 512 + pcol) * 8 : OOB,
          v4u{__float_as_uint(lc < 8 ? acc[0] : acc[2]), tag, __float_as_uint(lc < 8 ? acc[1] : acc[3]), tag});
  }
  STAMP(10);
  {
    const v2u um = ws_take(rp, moff, um4, tag, n);
    STAMPV(25, 0x1001 - n);
    poll_a(rp, ooff);
    const float ms = __uint_as_float(um.x), Mx = x32_max(x16_max(ms));
    const float w = __builtin_amdgcn_exp2f((ms - Mx) * 1.4426950408889634f);
    const float sc2 = w * __builtin_amdgcn_rcpf(x32_sum(x16_sum(w * __uint_as_float(um.y))));
    if (lp < TH_WO) lp = lds_spin_ge(F_LP, TH_WO);
    const v4u wf0 = tr_frag(L + RA + wb, 4096), wf1 = tr_frag(L + RA + wb + 8192, 4096);
    asm volatile("" ::"v"(sc2), "v"(wf0), "v"(wf1));
    poll_b(rp, ooff);
    const v2u uo = poll_end(rp, ooff, tag, n);
    STAMPV(22, 0x1000 - n);
    STAMP(11);
    const float o0 = x32_sum(x16_sum(sc2 * bflo(uo.x))), o1 = x32_sum(x16_sum(sc2 * bfhi(uo.x))),
                o2 = x32_sum(x16_sum(sc2 * bflo(uo.y))), o3 = x32_sum(x16_sum(sc2 * bfhi(uo.y)));
    if (lg == 0)
      *(LAS v2u*)(L + OL + (((lc >> 3) * 4 + (lc & 3)) * 8 + cw) * 16 + ((lc >> 2) & 1) * 8) = v2u{pk(o0, o1), pk(o2, o3)};
    asm volatile("s_waitcnt lgkmcnt(0)" ::: "memory");
    __builtin_amdgcn_s_barrier();
    asm volatile("" ::: "memory");
    f32x4 acc = mfma(wf0, *(LAS v4u*)(L + OL + (lg * 8 + (lc & 7)) * 16), f32x4{0.f, 0.f, 0.f, 0.f});
    acc = mfma(wf1, *(LAS v4u*)(L + OL + ((4 + lg) * 8 + (lc & 7)) * 16), acc);
    ws_st(rw, WS_SP + (((g8 * 8 + dt) * 8 + (lc & 7)) * 512 + pcol) * 8,
          v4u{__float_as_uint(lc < 8 ? acc[0] : acc[2]), tag, __float_as_uint(lc < 8 ? acc[1] : acc[3]), tag});
  }
  STAMP(12);
  {
    LAS float* f = (LAS float*)(L + FR) + (2 * fpr) * 8 + cw;
    const v4u ud4 = ws_ld(rw, doff);
    __builtin_amdgcn_sched_barrier(0);
    if (cw < 7) {
      const v2u ud = ws_take(rp, doff, ud4, tag, n);
      STAMPV(24, 0x1001 - n);
      poll_a(rp, soff);
      float d0 = __uint_as_float(ud.x), d1 = __uint_as_float(ud.y);
      d0 += dpp<XOR8>(d0); d1 += dpp<XOR8>(d1);
      d0 = x32_sum(x16_sum(d0)); d1 = x32_sum(x16_sum(d1));
      if (lane < 8) { f[128] = d0; f[136] = d1; }
      lds_arrive(F_D, lane);
    } else {
      asm volatile("s_waitcnt vmcnt(0)" ::: "memory");
      poll_a(rp, soff);
    }
    STAMP(13);
    __builtin_amdgcn_s_sleep(PGAP);
    poll_b(rp, soff);
    const v2u us = poll_end(rp, soff, tag, n);
    STAMPV(23, 0x1000 - n);
    STAMP(14);
    float s0 = __uint_as_float(us.x), s1 = __uint_as_float(us.y);
    s0 += dpp<XOR8>(s0); s1 += dpp<XOR8>(s1);
    s0 = x32_sum(x16_sum(s0)); s1 = x32_sum(x16_sum(s1));
    if (lane < 8) { f[0] = s0; f[8] = s1; }
    asm volatile("s_waitcnt lgkmcnt(0)" ::: "memory");
    __builtin_amdgcn_s_barrier();
    STAMP(15);
    STAMP_DUMP();
  }
}

extern "C" void kernel_launch(void* const* d_in, const int* in_sizes, int n_in, void* d_out, int out_size, void* d_ws,
                              size_t ws_size, hipStream_t stream) {
  fused_decode_block<<<256, 576, 0, stream>>>((const u16*)d_in[0], (const u16*)d_in[1], (const u16*)d_in[2], (const u16*)d_in[3],
                                              (const u16*)d_in[4], (const u16*)d_in[6], (const u16*)d_in[7], (const u16*)d_in[8],
                                              (u16*)d_out, (unsigned char*)d_ws);
}
```

```cpp
#define STAMPS 1
#include <cstdio>
#include <hip/hip_runtime.h>

typedef unsigned short u16;
typedef unsigned u32;
typedef unsigned long long u64;
typedef u32 v4u __attribute__((ext_vector_type(4)));
typedef u32 v2u __attribute__((ext_vector_type(2)));
typedef float f32x4 __attribute__((ext_vector_type(4)));
typedef short s16x4 __attribute__((ext_vector_type(4)));
typedef __bf16 bf16x8 __attribute__((ext_vector_type(8)));
typedef __bf16 bf16x2 __attribute__((ext_vector_type(2)));
#define LAS __attribute__((address_space(3)))
#define GAS __attribute__((address_space(1)))
typedef __amdgpu_buffer_rsrc_t rsrc_t;

namespace {
constexpr int kD = 512, kSK = 4096, kW0 = 3072, kF = 1792;
constexpr int RK = 0, RV = 65536, RWQ = RV + 49152, RA = 131072, RD = 147456, RM = 155648;
constexpr int XN = RM, OL = RM, FR = RM + 1024, QL = RM + 2048, PL = RM + 3328, MLM = RM + 5376, MLL = RM + 5504,
              HL = RM + 5632, FL = RM + 6144;
constexpr int F_LP = FL, F_XN = FL + 4, F_WQ = FL + 8, F_GU = FL + 12, F_Q = FL + 16, F_D = FL + 20, F_P = FL + 24, F_H = FL + 28;
constexpr int EL = FL + 64, ST = FL + 320, RX = RD;
constexpr int LDS_BYTES = 163840;
constexpr int WS_E = 0, WS_QP = 4096, WS_HP = WS_QP + 4 * 8 * 2048 * 8, WS_OP = WS_HP + 4 * 2 * 8 * 896 * 8,
              WS_ML = WS_OP + 8 * 32 * 4 * 64 * 8, WS_SP = WS_ML + 8 * 32 * 4 * 16, WS_DP = WS_SP + 64 * 8 * 512 * 8,
              WS_END = WS_DP + 56 * 8 * 512 * 8;
constexpr int OOB = 0x7ffffff0;
constexpr int AUX_NT = 2, AUX_SC1 = 16, RSRC3 = 0x00020000;
constexpr u32 kSpinLds = 1u << 22, kSpinGlb = 1u << 16;

__device__ __forceinline__ float bflo(u32 w) { return __builtin_bit_cast(float, w << 16); }
__device__ __forceinline__ float bfhi(u32 w) { return __builtin_bit_cast(float, w & 0xffff0000u); }
__device__ __forceinline__ u32 pk(float lo, float hi) {
  bf16x2 v = {(__bf16)lo, (__bf16)hi};
  return __builtin_bit_cast(u32, v);
}
__device__ __forceinline__ float rbf(float f) { return (float)(__bf16)f; }
__device__ __forceinline__ f32x4 mfma(v4u a, v4u b, f32x4 c) {
  return __builtin_amdgcn_mfma_f32_16x16x32_bf16(__builtin_bit_cast(bf16x8, a), __builtin_bit_cast(bf16x8, b), c, 0, 0, 0);
}
template <int CTRL> __device__ __forceinline__ float dpp(float x) {
  return __builtin_bit_cast(float, __builtin_amdgcn_mov_dpp(__builtin_bit_cast(int, x), CTRL, 0xf, 0xf, true));
}
constexpr int XOR1 = 0xB1, XOR2 = 0x4E, XOR7 = 0x141, XOR8 = 0x128, ROR4 = 0x124;
__device__ __forceinline__ float x16_sum(float x) {
  auto s = __builtin_amdgcn_permlane16_swap(__float_as_uint(x), __float_as_uint(x), false, false);
  return __uint_as_float(s[0]) + __uint_as_float(s[1]);
}
__device__ __forceinline__ float x32_sum(float x) {
  auto s = __builtin_amdgcn_permlane32_swap(__float_as_uint(x), __float_as_uint(x), false, false);
  return __uint_as_float(s[0]) + __uint_as_float(s[1]);
}
__device__ __forceinline__ float x16_max(float x) {
  auto s = __builtin_amdgcn_permlane16_swap(__float_as_uint(x), __float_as_uint(x), false, false);
  return fmaxf(__uint_as_float(s[0]), __uint_as_float(s[1]));
}
__device__ __forceinline__ float x32_max(float x) {
  auto s = __builtin_amdgcn_permlane32_swap(__float_as_uint(x), __float_as_uint(x), false, false);
  return fmaxf(__uint_as_float(s[0]), __uint_as_float(s[1]));
}
__device__ __forceinline__ float wave_sum(float x) {
  x += dpp<XOR1>(x); x += dpp<XOR2>(x); x += dpp<XOR7>(x); x += dpp<XOR8>(x);
  return x32_sum(x16_sum(x));
}
__device__ __forceinline__ float silu(float x) { return x * __builtin_amdgcn_rcpf(1.f + __builtin_amdgcn_exp2f(x * -1.4426950408889634f)); }

__device__ __forceinline__ void lds_set(int addr, u32 val) {
  asm volatile("ds_write_b32 %0, %1" ::"v"(addr), "v"(val) : "memory");
}
__device__ __forceinline__ u32 lds_get(int addr) {
  u32 r;
  asm volatile("ds_read_b32 %0, %1\n\ts_waitcnt lgkmcnt(0)" : "=v"(r) : "v"(addr) : "memory");
  return __builtin_amdgcn_readfirstlane(r);
}
__device__ __forceinline__ u32 lds_wait_ge(int addr, u32 want) {
  u32 v = lds_get(addr);
  for (u32 n = 0; v < want && n < kSpinLds; ++n) { __builtin_amdgcn_s_sleep(1); v = lds_get(addr); }
  return v;
}
__device__ __forceinline__ u32 lds_spin_ge(int addr, u32 want) {
  u32 v = lds_get(addr);
  for (u32 n = 0; v < want && n < kSpinLds; ++n) v = lds_get(addr);
  return v;
}
__device__ __forceinline__ void lds_arrive(int addr, int) {
  asm volatile("s_mov_b64 exec, 1\n\tds_add_u32 %0, %1\n\ts_mov_b64 exec, -1" ::"v"(addr), "v"(1u) : "memory");
}
__device__ __forceinline__ v4u ws_ld(rsrc_t rs, int off) { return __builtin_amdgcn_raw_buffer_load_b128(rs, off, 0, AUX_SC1); }
__device__ __forceinline__ void ws_st(rsrc_t rs, int off, v4u v) { __builtin_amdgcn_raw_buffer_store_b128(v, rs, off, 0, AUX_SC1); }
__device__ __forceinline__ bool tag_ok(v4u u, u32 tag) { return u.y == tag && u.w == tag; }
__device__ __forceinline__ bool all_of(bool ok) { return __builtin_amdgcn_ballot_w64(!ok) == 0ull; }
typedef int s4i __attribute__((ext_vector_type(4)));
#define POLL_REGS "v96", "v97", "v98", "v99", "v100", "v101", "v102", "v103", "v104", "v105", "v106", "v107"
__device__ __forceinline__ void poll_a(s4i rs, int off) {
  asm volatile("buffer_load_dwordx4 v[96:99], %0, %1, 0 offen sc1" ::"v"(off), "s"(rs) : "memory", POLL_REGS);
}
__device__ __forceinline__ void poll_b(s4i rs, int off) {
  asm volatile("buffer_load_dwordx4 v[100:103], %0, %1, 0 offen sc1" ::"v"(off), "s"(rs) : "memory", POLL_REGS);
}
__device__ __forceinline__ void poll_c(s4i rs, int off) {
  asm volatile("buffer_load_dwordx4 v[104:107], %0, %1, 0 offen sc1" ::"v"(off), "s"(rs) : "memory", POLL_REGS);
}
__device__ __forceinline__ v4u take_c() {
  v4u u;
  asm volatile("s_waitcnt vmcnt(2)\n\tv_mov_b32 %0, v104\n\tv_mov_b32 %1, v105\n\tv_mov_b32 %2, v106\n\tv_mov_b32 %3, v107"
               : "=v"(u.x), "=v"(u.y), "=v"(u.z), "=v"(u.w)::"memory", POLL_REGS);
  return u;
}
__device__ __forceinline__ v2u poll_end(s4i rs, int off, u32 tag, u32& left) {
  u32 x, z, cnt;
  int ad;
  u64 ok, t;
#define POLL_STEP(r0, r1, r2, r3)                                                                   \
  "s_waitcnt vmcnt(1)\n\t"                                                                          \
  "v_cmp_eq_u32_e32 vcc, %[tag], v" #r1 "\n\t"                                                      \
  "v_cmp_eq_u32_e64 %[t], %[tag], v" #r3 "\n\t"                                                     \
  "s_and_b64 vcc, vcc, %[t]\n\t"                                                                    \
  "s_andn2_b64 vcc, vcc, %[ok]\n\t"                                                                 \
  "v_cndmask_b32_e32 %[x], %[x], v" #r0 ", vcc\n\t"                                                 \
  "v_cndmask_b32_e32 %[z], %[z], v" #r2 ", vcc\n\t"                                                 \
  "s_or_b64 %[ok], %[ok], vcc\n\t"                                                                  \
  "s_cmp_eq_u64 %[ok], -1\n\t"                                                                      \
  "s_cbranch_scc1 .Lpoll_done_%=\n\t"                                                               \
  "v_cndmask_b32_e64 %[ad], %[off], %[oob], %[ok]\n\t"                                              \
  "buffer_load_dwordx4 v[" #r0 ":" #r3 "], %[ad], %[rs], 0 offen sc1\n\t"
  asm volatile("s_mov_b64 %[ok], 0\n\t"
               "s_movk_i32 %[cnt], 0x1000\n"
               ".Lpoll_loop_%=:\n\t" POLL_STEP(96, 97, 98, 99) POLL_STEP(100, 101, 102, 103)
               "s_sub_u32 %[cnt], %[cnt], 1\n\t"
               "s_cmp_lg_u32 %[cnt], 0\n\t"
               "s_cbranch_scc1 .Lpoll_loop_%=\n"
               ".Lpoll_done_%=:\n\t"
               "s_nop 0"
               : [x] "=&v"(x), [z] "=&v"(z), [ad] "=&v"(ad), [ok] "=&s"(ok), [t] "=&s"(t), [cnt] "=&s"(cnt)
               : [off] "v"(off), [oob] "v"(OOB), [rs] "s"(rs), [tag] "s"(tag)
               : "memory", "vcc", "scc", POLL_REGS);
  left = cnt;
  return v2u{x, z};
}
__device__ __forceinline__ v2u ws_take(s4i rs, int off, v4u u, u32 tag, u32& n) {
  n = 0x1001;
  if (all_of(tag_ok(u, tag))) return v2u{u.x, u.z};
  poll_a(rs, off);
  __builtin_amdgcn_s_sleep(4);
  poll_b(rs, off);
  return poll_end(rs, off, tag, n);
}
__device__ __forceinline__ u32 now() { return (u32)__builtin_amdgcn_s_memrealtime(); }
__device__ __forceinline__ void wait_until(u32 t0, u32 dt) {
  while (now() - t0 < dt) __builtin_amdgcn_s_sleep(1);
}
__device__ __forceinline__ v4u tr_frag(LAS unsigned char* p, int half) {
  const v2u a = __builtin_bit_cast(v2u, __builtin_amdgcn_ds_read_tr16_b64_v4i16((LAS s16x4*)p));
  const v2u b = __builtin_bit_cast(v2u, __builtin_amdgcn_ds_read_tr16_b64_v4i16((LAS s16x4*)(p + half)));
  return v4u{a.x, a.y, b.x, b.y};
}
}

#define LAG 5
#define LAGK 5
#define SPDELAY 8
#define TQ1 350
#define TQ2 430
#define ODELAY 8
#define PGAP 3
#define QPOLL 6
#define HPOLL 9
#define STAMP(i) do { u64 t_; asm volatile("s_memrealtime %0\n\ts_waitcnt lgkmcnt(0)" : "=s"(t_)::"memory"); lds_set(ST + (wave * 32 + (i)) * 4, (u32)t_); } while (0)
#define STAMPV(i, v) lds_set(ST + (wave * 32 + (i)) * 4, (u32)(v))
#define STAMP_DUMP() do { asm volatile("s_waitcnt lgkmcnt(0)" ::: "memory"); if (lane < 32) ((GAS u64*)(ws + (64 << 20)))[(blockIdx.x * 10 + wave) * 32 + lane] = *(LAS u32*)(L + ST + (wave * 32 + lane) * 4); } while (0)

__global__ void __launch_bounds__(640, 3)
fused_decode_block(const u16* __restrict__ x, const u16* __restrict__ kc, const u16* __restrict__ vc, const u16* __restrict__ wq,
                   const u16* __restrict__ wo, const u16* __restrict__ wg, const u16* __restrict__ wu, const u16* __restrict__ wd,
                   u16* __restrict__ out, unsigned char* __restrict__ ws) {
  __shared__ __attribute__((aligned(16))) unsigned char lds[LDS_BYTES];
  LAS unsigned char* L = (LAS unsigned char*)lds;
  const int tid = threadIdx.x, lane = tid & 63, wave = __builtin_amdgcn_readfirstlane(tid >> 6);
  asm volatile("" ::"s"(x), "s"(kc), "s"(vc), "s"(wq), "s"(wo), "s"(wg), "s"(wu), "s"(wd), "s"(out), "s"(ws));
  const u32 t0 = now();
  const int c = blockIdx.x, g8 = c & 7, r = c >> 3;
  const int fb = c >> 5, fc = (c & 31) * 16;
  const int j = c >> 6, t64 = c & 63;
  const bool has_gu = t64 < 56;
  const int fg = (r >> 2) * 8 + g8, cg = r & 3;
  const bool has_dn = fg < 56;
  const int dt = r >> 2;
  const int ab = r >> 2, as = r & 3;
  const rsrc_t rw = __builtin_amdgcn_make_buffer_rsrc((void*)ws, 0, WS_END, RSRC3);
  const s4i rp = {(int)(u32)(u64)ws, (int)((u32)((u64)ws >> 32) & 0xffffu), WS_END, RSRC3};
  const u32 TH_K = has_gu ? 5 : 3, TH_V = TH_K + 8, TH_DN = TH_V + 9, TH_WO = TH_V + 8 + (has_dn ? 1 : 0) + 2;
  if (lane < 32) lds_set(ST + (wave * 32 + lane) * 4, 0xffffffffu);
  STAMP(0);

  if (__builtin_expect(wave == 0, 1)) {
    const int r8 = lane >> 3, r4 = lane >> 4;
    const int sw128 = ((lane & 7) ^ ((r4 & 3) << 1)) * 16;
    const int swV = ((lane & 15) ^ (r4 << 1)) * 16;
    const int swK = ((lane & 15) ^ r4) * 16;
    u32 cnt, lpv = 1;
    __builtin_amdgcn_s_setprio(3);
#define GLDS(rs, ldsoff, voff, soff) __builtin_amdgcn_raw_ptr_buffer_load_lds(rs, (LAS void*)(L + (ldsoff)), 16, voff, soff, 0, AUX_NT)
#define GROUP_DONE_L(LG) do { asm volatile("s_waitcnt vmcnt(%0)" ::"n"((LG) * 8) : "memory"); ++cnt; if (cnt + 1 > lpv + (LG)) { lpv = cnt + 1 - (LG); lds_set(F_LP, lpv); } } while (0)
#define GROUP_DONE() GROUP_DONE_L(LAG)
    {
      const rsrc_t rs = __builtin_amdgcn_make_buffer_rsrc((void*)x, 0, 8192, RSRC3);
#pragma unroll
      for (int i = 0; i < 8; ++i) __builtin_amdgcn_raw_ptr_buffer_load_lds(rs, (LAS void*)(L + RX + i * 1024), 16, lane * 16, i * 1024, 0, 0);
      __builtin_amdgcn_raw_ptr_buffer_load_lds(rw, (LAS void*)(L + EL), 4, lane * 4, WS_E, 0, AUX_SC1);
    }
    {
      const rsrc_t rs = __builtin_amdgcn_make_buffer_rsrc((void*)wq, 0, 512 * 4096 * 2, RSRC3);
      const int vo = r8 * 8192 + sw128, so = j * 128 * 8192 + t64 * 128;
#pragma unroll
      for (int i = 0; i < 16; ++i) GLDS(rs, RWQ + i * 1024, vo, so + i * 8 * 8192);
    }
    STAMP(1);
    if (has_gu) {
      const rsrc_t rs = __builtin_amdgcn_make_buffer_rsrc((void*)(t64 < 28 ? wg : wu), 0, 512 * kF * 2, RSRC3);
      const int vo = r8 * (kF * 2) + sw128, so = j * 128 * (kF * 2) + (t64 < 28 ? t64 : t64 - 28) * 128;
#pragma unroll
      for (int i = 0; i < 16; ++i) GLDS(rs, RA + i * 1024, vo, so + i * 8 * (kF * 2));
      __builtin_amdgcn_s_barrier();
      asm volatile("s_waitcnt vmcnt(32)" ::: "memory");
      cnt = 4;
    } else {
      __builtin_amdgcn_s_barrier();
      asm volatile("s_waitcnt vmcnt(16)" ::: "memory");
      cnt = 2;
    }
    lds_set(F_LP, 1u);
    u32 xfin;
    asm volatile("ds_read_b32 %0, %1" : "=v"(xfin) : "v"(RX + (fb * kD + fc + (lane & 7) * 2) * 2) : "memory");
    const u32 tag = lds_get(EL) + 1u;
    asm volatile("" : "+v"(xfin));
    STAMP(2);
    const size_t kvoff = ((size_t)(ab * 8 + g8) * kSK + kW0 + as * 256) * 128;
    {
      const rsrc_t rs = __builtin_amdgcn_make_buffer_rsrc((void*)(kc + kvoff), 0, 65536, RSRC3);
      const int vo = r4 * 256;
#pragma unroll 1
      for (int n = 0; n < 8; ++n) {
#pragma unroll
        for (int i = 0; i < 8; ++i) GLDS(rs, RK + n * 8192 + i * 1024, vo + (swK ^ ((i & 3) * 64)), n * 8192 + i * 1024);
        GROUP_DONE_L(LAGK);
      }
    }
    STAMP(3);
    {
      const rsrc_t rs = __builtin_amdgcn_make_buffer_rsrc((void*)(vc + kvoff), 0, 65536, RSRC3);
      const int vo = r4 * 256;
#pragma unroll 1
      for (int n = 0; n < 8; ++n) {
        if (n == 6) lds_wait_ge(F_WQ, 4);
#pragma unroll
        for (int i = 0; i < 8; ++i) GLDS(rs, RV + n * 8192 + i * 1024, vo + (swV ^ ((i & 1) * 128)), n * 8192 + i * 1024);
        GROUP_DONE();
      }
    }
    STAMP(4);
    if (has_dn) {
      const rsrc_t rs = __builtin_amdgcn_make_buffer_rsrc((void*)wd, 0, kF * 512 * 2, RSRC3);
      const int vo = r4 * 1024, so = fg * 32 * 1024 + cg * 256;
#pragma unroll
      for (int i = 0; i < 8; ++i) GLDS(rs, RD + i * 1024, vo + (swV ^ ((i & 1) * 128)), so + i * 4096);
      GROUP_DONE();
    }
    if (has_gu) lds_wait_ge(F_GU, 4);
    {
      const rsrc_t rs = __builtin_amdgcn_make_buffer_rsrc((void*)wo, 0, 4096 * 512 * 2, RSRC3);
      const int vo = r4 * 1024, so = (g8 * 512 + dt * 16) * 1024 + cg * 256;
#pragma unroll
      for (int n = 0; n < 2; ++n) {
#pragma unroll
        for (int i = 0; i < 8; ++i)
          GLDS(rs, RA + n * 8192 + i * 1024, vo + (swV ^ ((i & 1) * 128)), so + ((2 * n + (i >> 2)) * 128 + (i & 3) * 4) * 1024);
        GROUP_DONE();
      }
    }
    STAMP(5);
    asm volatile("s_waitcnt vmcnt(32)" ::: "memory"); lds_set(F_LP, cnt - 3);
    asm volatile("s_waitcnt vmcnt(24)" ::: "memory"); lds_set(F_LP, cnt - 2);
    STAMP(6);
    asm volatile("s_waitcnt vmcnt(16)" ::: "memory"); lds_set(F_LP, cnt - 1);
    asm volatile("s_waitcnt vmcnt(8)" ::: "memory");  lds_set(F_LP, cnt);
    asm volatile("s_waitcnt vmcnt(0)" ::: "memory");  lds_set(F_LP, cnt + 1);
    STAMP(7);
    __builtin_amdgcn_s_barrier();
    float f0, f1;
    {
      const int doff = WS_DP + (((lane >> 3) * 8 + fb) * 512 + fc + (lane & 7) * 2) * 8;
      for (u32 n = 0; n < kSpinGlb; ++n) {
        asm volatile("" ::: "memory");
        v4u u[7];
#pragma unroll
        for (int i = 0; i < 7; ++i) u[i] = ws_ld(rw, doff + i * (8 * 8 * 512 * 8));
        bool ok = true;
        f0 = 0.f; f1 = 0.f;
#pragma unroll
        for (int i = 0; i < 7; ++i) { ok = ok && tag_ok(u[i], tag); f0 += __uint_as_float(u[i].x); f1 += __uint_as_float(u[i].z); }
        STAMPV(20, n);
        if (all_of(ok)) break;
      }
      f0 += dpp<XOR8>(f0); f1 += dpp<XOR8>(f1);
      f0 = rbf(silu(rbf(x32_sum(x16_sum(f0)))));
      f1 = rbf(silu(rbf(x32_sum(x16_sum(f1)))));
      asm volatile("" ::"v"(f0), "v"(f1));
    }
    STAMP(8);
    __builtin_amdgcn_s_barrier();
    asm volatile("" ::: "memory");
    LAS f32x4* fr = (LAS f32x4*)(L + FR + (lane & 7) * 64);
    const f32x4 a0 = fr[0], a1 = fr[1], a2 = fr[2], a3 = fr[3];
    const float s0 = rbf(((a0[0] + a0[1]) + (a0[2] + a0[3])) + ((a1[0] + a1[1]) + (a1[2] + a1[3])));
    const float s1 = rbf(((a2[0] + a2[1]) + (a2[2] + a2[3])) + ((a3[0] + a3[1]) + (a3[2] + a3[3])));
    if (lane < 8) ((GAS u32*)out)[(fb * kD + fc) / 2 + lane] = pk(bflo(xfin) + rbf(s0 * f0), bfhi(xfin) + rbf(s1 * f1));
    if (c == 0 && lane == 0) __builtin_amdgcn_raw_buffer_store_b32(tag, rw, WS_E, 0, AUX_SC1);
    STAMP(9);
    STAMP_DUMP();
    return;
  }

  const int cw = wave - 1, lc = lane & 15, lg = lane >> 4;
  if (cw == 0 && lane < 8) lds_set(FL + lane * 4, 0u);
  asm volatile("s_waitcnt lgkmcnt(0)" ::: "memory");
  __builtin_amdgcn_s_barrier();
  const int tq = lc >> 2, tp = lc & 3, row0 = 4 * lg + tq;
  if (wave == 9) {
    if (!has_dn) return;
    wait_until(t0, TQ2);
    lds_wait_ge(F_LP, 1);
    const u32 tag = lds_get(EL) + 1u;
    const int hoff = WS_HP + ((lane >> 3) * 896 + fg * 16 + (lane & 7) * 2) * 8;
    float g[4], u[4];
    for (u32 n = 0; n < kSpinGlb; ++n) {
      asm volatile("" ::: "memory");
      v4u t[8];
#pragma unroll
      for (int i = 0; i < 8; ++i) t[i] = ws_ld(rw, hoff + i * (8 * 896 * 8));
      bool ok = true;
#pragma unroll
      for (int i = 0; i < 8; ++i) ok = ok && tag_ok(t[i], tag);
      g[0] = (bflo(t[0].x) + bflo(t[2].x)) + (bflo(t[4].x) + bflo(t[6].x)); u[0] = (bflo(t[1].x) + bflo(t[3].x)) + (bflo(t[5].x) + bflo(t[7].x));
      g[1] = (bfhi(t[0].x) + bfhi(t[2].x)) + (bfhi(t[4].x) + bfhi(t[6].x)); u[1] = (bfhi(t[1].x) + bfhi(t[3].x)) + (bfhi(t[5].x) + bfhi(t[7].x));
      g[2] = (bflo(t[0].z) + bflo(t[2].z)) + (bflo(t[4].z) + bflo(t[6].z)); u[2] = (bflo(t[1].z) + bflo(t[3].z)) + (bflo(t[5].z) + bflo(t[7].z));
      g[3] = (bfhi(t[0].z) + bfhi(t[2].z)) + (bfhi(t[4].z) + bfhi(t[6].z)); u[3] = (bfhi(t[1].z) + bfhi(t[3].z)) + (bfhi(t[5].z) + bfhi(t[7].z));
      STAMPV(20, n);
      if (all_of(ok)) break;
    }
#pragma unroll
    for (int i = 0; i < 4; ++i) g[i] = rbf(silu(rbf(g[i]))) * rbf(u[i]);
    *(LAS v2u*)(L + HL + ((lane & 3) * 8 + (lane >> 3)) * 16 + ((lane >> 2) & 1) * 8) = v2u{pk(g[0], g[1]), pk(g[2], g[3])};
    const v4u hf = *(LAS v4u*)(L + HL + (lg * 8 + (lc & 7)) * 16);
    const int off = WS_DP + ((fg * 8 + (lc & 7)) * 512 + cg * 128 + lg * 4 + (lc >> 3) * 2) * 8;
    STAMP(1);
    lds_wait_ge(F_LP, TH_DN);
    STAMP(2);
#pragma unroll
    for (int i = 0; i < 8; ++i) {
      const f32x4 acc = mfma(tr_frag(L + RD + row0 * 256 + (((2 * i + (tp >> 1)) ^ ((row0 & 7) << 1)) * 16) + (tp & 1) * 8, 4096), hf,
                             f32x4{0.f, 0.f, 0.f, 0.f});
      ws_st(rw, off + i * 128, v4u{__float_as_uint(lc < 8 ? acc[0] : acc[2]), tag, __float_as_uint(lc < 8 ? acc[1] : acc[3]), tag});
    }
    STAMP(3);
    STAMP_DUMP();
    return;
  }
  u32 lp = lds_spin_ge(F_LP, 1);
  STAMP(1);
  const u32 tag = lds_get(EL) + 1u;
  {
    const v4u xr = *(LAS v4u*)(L + RX + cw * 1024 + lane * 16);
    float v[8];
#pragma unroll
    for (int i = 0; i < 4; ++i) { v[2 * i] = bflo(xr[i]); v[2 * i + 1] = bfhi(xr[i]); }
    const float x0 = __builtin_bit_cast(float, __builtin_amdgcn_readfirstlane(xr[0] << 16));
    float s0 = 0.f, q0 = 0.f;
#pragma unroll
    for (int i = 0; i < 8; ++i) { const float d = v[i] - x0; s0 += d; q0 = fmaf(d, d, q0); }
    s0 = wave_sum(s0) * (1.f / kD);
    q0 = wave_sum(q0) * (1.f / kD);
    const float mb = rbf(x0 + s0), vb = rbf(fmaxf(fmaf(-s0, s0, q0), 0.f));
    const float rden = __builtin_amdgcn_rcpf(rbf(__builtin_amdgcn_sqrtf(rbf(vb + 1e-5f))));
#pragma unroll
    for (int i = 0; i < 8; ++i) v[i] = rbf(v[i] - mb) * rden;
    if (lg == j) {
      const int ks = lc >> 2, o = lc & 3, gq = (o & 1) * 2, hf = o >> 1;
      LAS v2u* p = (LAS v2u*)(L + XN + ((ks * 4 + gq) * 8 + cw) * 16 + hf * 8);
      p[0] = v2u{pk(v[0], v[1]), pk(v[2], v[3])};
      p[16] = v2u{pk(v[4], v[5]), pk(v[6], v[7])};
    }
    lds_arrive(F_XN, lane);
  }
  STAMP(2);
  if (cw < 4 || has_gu) {
    const int nt = cw & 3;
    lds_spin_ge(F_XN, 8);
    v4u xf[4];
#pragma unroll
    for (int ks = 0; ks < 4; ++ks) xf[ks] = *(LAS v4u*)(L + XN + ((ks * 4 + lg) * 8 + (lc & 7)) * 16);
    lp = lds_spin_ge(F_LP, cw < 4 ? 3u : 5u);
    STAMP(3);
    LAS unsigned char* base = L + (cw < 4 ? RWQ : RA) + row0 * 128 + (((2 * nt + (tp >> 1)) ^ (((row0 >> 1) & 3) << 1)) * 16) + (tp & 1) * 8;
    f32x4 acc = {0.f, 0.f, 0.f, 0.f};
#pragma unroll
    for (int ks = 0; ks < 4; ++ks) acc = mfma(tr_frag(base + ks * 4096, 2048), xf[ks], acc);
    if (lc < 8) {
      const v4u o = {pk(acc[0], acc[1]), tag, pk(acc[2], acc[3]), tag};
      const int off = cw < 4 ? WS_QP + ((j * 8 + lc) * 2048 + t64 * 32 + nt * 8 + lg * 2) * 8
                             : WS_HP + (((j * 2 + (t64 >= 28)) * 8 + lc) * 896 + (t64 >= 28 ? t64 - 28 : t64) * 32 + nt * 8 + lg * 2) * 8;
      ws_st(rw, off, o);
    }
    lds_arrive(cw < 4 ? F_WQ : F_GU, lane);
    STAMP(4);
  }
  u32 n;
  {
    const int qoff = WS_QP + ((lg * 8 + ab) * 2048 + g8 * 256 + (cw * 16 + lc) * 2) * 8;
    wait_until(t0, TQ1);
    STAMP(5);
    const v4u uq4 = ws_ld(rw, qoff);
    const v2u uq = ws_take(rp, qoff, uq4, tag, n);
    STAMPV(20, 0x1001 - n);
    STAMP(6);
    const float q0 = x32_sum(x16_sum(bflo(uq.x))), q1 = x32_sum(x16_sum(bfhi(uq.x))), q2 = x32_sum(x16_sum(bflo(uq.y))),
                q3 = x32_sum(x16_sum(bfhi(uq.y)));
    if (lg == 0) {
      const int d = cw * 64 + lc * 4;
      *(LAS v2u*)(L + QL + (d >> 7) * 272 + (d & 127) * 2) = v2u{pk(q0, q1), pk(q2, q3)};
    }
    lds_arrive(F_Q, lane);
  }
  {
    if (lp < TH_K + cw + 1) lp = lds_wait_ge(F_LP, TH_K + cw + 1);
    v4u kf[8];
#pragma unroll
    for (int i = 0; i < 8; ++i) kf[i] = *(LAS v4u*)(L + RK + (cw * 32 + (i >> 2) * 16 + lc) * 256 + ((((i & 3) * 4 + lg) ^ lc) * 16));
    lds_spin_ge(F_Q, 8);
    STAMP(7);
    v4u qf[4];
#pragma unroll
    for (int ks = 0; ks < 4; ++ks) qf[ks] = *(LAS v4u*)(L + QL + (lc & 3) * 272 + ks * 64 + lg * 16);
    f32x4 a0 = {0.f, 0.f, 0.f, 0.f}, a1 = {0.f, 0.f, 0.f, 0.f};
#pragma unroll
    for (int ks = 0; ks < 4; ++ks) { a0 = mfma(kf[ks], qf[ks], a0); a1 = mfma(kf[4 + ks], qf[ks], a1); }
    float sc[8];
#pragma unroll
    for (int i = 0; i < 4; ++i) { sc[i] = rbf(rbf(a0[i]) * 0.08838834764831845f); sc[4 + i] = rbf(rbf(a1[i]) * 0.08838834764831845f); }
    float m = fmaxf(fmaxf(fmaxf(sc[0], sc[1]), fmaxf(sc[2], sc[3])), fmaxf(fmaxf(sc[4], sc[5]), fmaxf(sc[6], sc[7])));
    m = x32_max(x16_max(m));
#pragma unroll
    for (int i = 0; i < 8; ++i) sc[i] = __builtin_amdgcn_exp2f((sc[i] - m) * 1.4426950408889634f);
    if (lc < 4) *(LAS v4u*)(L + PL + ((cw * 4 + lg) * 4 + lc) * 16) = v4u{pk(sc[0], sc[1]), pk(sc[2], sc[3]), pk(sc[4], sc[5]), pk(sc[6], sc[7])};
    const float l = x32_sum(x16_sum(((sc[0] + sc[1]) + (sc[2] + sc[3])) + ((sc[4] + sc[5]) + (sc[6] + sc[7]))));
    if (lane < 4) { *(LAS float*)(L + MLM + (lane * 8 + cw) * 4) = m; *(LAS float*)(L + MLL + (lane * 8 + cw) * 4) = l; }
    lds_arrive(F_P, lane);
  }
  lds_spin_ge(F_P, 8);
  STAMP(8);
  const int wb = row0 * 256 + (((2 * cw + (tp >> 1)) ^ ((row0 & 7) << 1)) * 16) + (tp & 1) * 8;
  int opw = WS_OP + ((((g8 * 8 + cw) * 8 + ab) * 4 + as) * 16 + lc * 4 + lg) * 16;
  int ooff = WS_OP + ((((g8 * 8 + dt) * 8 + cw) * 4 + lg) * 16 + lc) * 16;
  int moff = WS_ML + (((cw * 8 + g8) * 4 + lg) * 4 + (lc >> 2)) * 16;
  int spw = WS_SP + (((g8 * 8 + dt) * 8 + (lc & 7)) * 512 + cg * 128 + cw * 16 + lg * 4 + (lc >> 3) * 2) * 8;
  int soff = WS_SP + (((cw * 8 + (lane >> 3)) * 8 + fb) * 512 + fc + (lane & 7) * 2) * 8;
  int olw = OL + (((lc >> 3) * 4 + (lc & 3)) * 8 + cw) * 16 + ((lc >> 2) & 1) * 8, olr = OL + (lg * 8 + (lc & 7)) * 16;
  int frw = FR + (lane & 7) * 64 + cw * 4;
  {
    const f32x4 m0 = *(LAS f32x4*)(L + MLM + (lc & 3) * 32), m1 = *(LAS f32x4*)(L + MLM + (lc & 3) * 32 + 16);
    v4u pf[8];
#pragma unroll
    for (int i = 0; i < 8; ++i) pf[i] = *(LAS v4u*)(L + PL + ((i * 4 + lg) * 4 + (lc & 3)) * 16);
    const float M = fmaxf(fmaxf(fmaxf(m0[0], m0[1]), fmaxf(m0[2], m0[3])), fmaxf(fmaxf(m1[0], m1[1]), fmaxf(m1[2], m1[3])));
    float f[8];
#pragma unroll
    for (int i = 0; i < 4; ++i) {
      f[i] = __builtin_amdgcn_exp2f((m0[i] - M) * 1.4426950408889634f);
      f[4 + i] = __builtin_amdgcn_exp2f((m1[i] - M) * 1.4426950408889634f);
    }
    if (cw == 0 && lane < 4) {
      const f32x4 l0 = *(LAS f32x4*)(L + MLL + lane * 32), l1 = *(LAS f32x4*)(L + MLL + lane * 32 + 16);
      const float l = (fmaf(l0[0], f[0], l0[1] * f[1]) + fmaf(l0[2], f[2], l0[3] * f[3])) + (fmaf(l1[0], f[4], l1[1] * f[5]) + fmaf(l1[2], f[6], l1[3] * f[7]));
      ws_st(rw, WS_ML + (((ab * 8 + g8) * 4 + as) * 4 + lane) * 16, v4u{__float_as_uint(M), tag, __float_as_uint(l), tag});
    }
    const f32x4 z = {0.f, 0.f, 0.f, 0.f};
    f32x4 acc = z;
    asm volatile("" : "+v"(ooff), "+v"(moff), "+v"(soff), "+v"(spw), "+v"(opw), "+v"(olw), "+v"(olr), "+v"(frw));
    if (lp < TH_V + 5) lp = lds_wait_ge(F_LP, TH_V + 5);
#pragma unroll
    for (int i = 0; i < 5; ++i) acc += f[i] * mfma(tr_frag(L + RV + wb + i * 8192, 4096), pf[i], z);
#pragma unroll
    for (int i = 5; i < 8; ++i) {
      if (lp < TH_V + i + 1) lp = lds_spin_ge(F_LP, TH_V + i + 1);
      acc += f[i] * mfma(tr_frag(L + RV + wb + i * 8192, 4096), pf[i], z);
    }
    if (lc < 4)
      ws_st(rw, opw, v4u{pk(acc[0], acc[1]), tag, pk(acc[2], acc[3]), tag});
    poll_c(rp, moff);
    STAMP(9);
  }
  {
    __builtin_amdgcn_s_sleep(ODELAY);
    STAMP(10);
    poll_a(rp, ooff);
    __builtin_amdgcn_s_sleep(PGAP);
    poll_b(rp, ooff);
    v4u um = take_c();
    if (__builtin_expect(!all_of(tag_ok(um, tag)), 0)) {
      n = 0;
      do {
        asm volatile("" ::: "memory");
        um = ws_ld(rw, moff);
      } while (!all_of(tag_ok(um, tag)) && ++n < kSpinGlb);
      STAMPV(25, n + 1);
    }
    const float ms = __uint_as_float(um.x), Mx = x32_max(x16_max(ms));
    const float w = __builtin_amdgcn_exp2f((ms - Mx) * 1.4426950408889634f);
    const float sc2 = w * __builtin_amdgcn_rcpf(x32_sum(x16_sum(w * __uint_as_float(um.z))));
    lds_spin_ge(F_LP, TH_WO);
    const v4u wf0 = tr_frag(L + RA + wb, 4096), wf1 = tr_frag(L + RA + wb + 8192, 4096);
    asm volatile("" ::"v"(sc2), "v"(wf0), "v"(wf1));
    const v2u uo = poll_end(rp, ooff, tag, n);
    STAMPV(22, 0x1000 - n);
    STAMP(11);
    const float o0 = x32_sum(x16_sum(sc2 * bflo(uo.x))), o1 = x32_sum(x16_sum(sc2 * bfhi(uo.x))),
                o2 = x32_sum(x16_sum(sc2 * bflo(uo.y))), o3 = x32_sum(x16_sum(sc2 * bfhi(uo.y)));
    if (lg == 0)
      *(LAS v2u*)(L + olw) = v2u{pk(o0, o1), pk(o2, o3)};
    asm volatile("s_waitcnt lgkmcnt(0)" ::: "memory");
    __builtin_amdgcn_s_barrier();
    asm volatile("" ::: "memory");
    f32x4 acc = mfma(wf0, *(LAS v4u*)(L + olr), f32x4{0.f, 0.f, 0.f, 0.f});
    acc = mfma(wf1, *(LAS v4u*)(L + olr + 512), acc);
    ws_st(rw, spw, v4u{__float_as_uint(lc < 8 ? acc[0] : acc[2]), tag, __float_as_uint(lc < 8 ? acc[1] : acc[3]), tag});
  }
  STAMP(12);
  {
    __builtin_amdgcn_s_sleep(SPDELAY);
    STAMP(13);
    poll_a(rp, soff);
    __builtin_amdgcn_s_sleep(PGAP);
    poll_b(rp, soff);
    const v2u us = poll_end(rp, soff, tag, n);
    STAMPV(23, 0x1000 - n);
    STAMP(14);
    float s0 = __uint_as_float(us.x), s1 = __uint_as_float(us.y);
    s0 += dpp<XOR8>(s0); s1 += dpp<XOR8>(s1);
    s0 = x32_sum(x16_sum(s0)); s1 = x32_sum(x16_sum(s1));
    if (lane < 8) { *(LAS float*)(L + frw) = s0; *(LAS float*)(L + frw + 32) = s1; }
    asm volatile("s_waitcnt lgkmcnt(0)" ::: "memory");
    __builtin_amdgcn_s_barrier();
    STAMP(15);
    STAMP_DUMP();
  }
}

__global__ void dbg_print(unsigned char* ws) {
  const u64* st = (const u64*)(ws + (64 << 20));
  u32 t0 = 0xffffffffu;
  for (int c = 0; c < 256; ++c) t0 = min(t0, (u32)st[(c * 10) * 32]);
  for (int g = 0; g < 2; ++g)
    for (int s = 0; s < 16; ++s) {
      u32 mn = 0xffffffffu, mx = 0; u64 sum = 0; int n = 0;
      for (int c = 0; c < 256; ++c) { u32 v = (u32)st[(c * 10 + g) * 32 + s]; if (v == 0xffffffffu) continue; v -= t0; mn = min(mn, v); mx = max(mx, v); sum += v; ++n; }
      if (n) printf("DBG w%d s%d min %u avg %u max %u (x10ns)\n", g, s, mn, (u32)(sum / n), mx);
    }
}
extern "C" void kernel_launch(void* const* d_in, const int* in_sizes, int n_in, void* d_out, int out_size, void* d_ws,
                              size_t ws_size, hipStream_t stream) {
  fused_decode_block<<<256, 640, 0, stream>>>((const u16*)d_in[0], (const u16*)d_in[1], (const u16*)d_in[2], (const u16*)d_in[3],
                                              (const u16*)d_in[4], (const u16*)d_in[6], (const u16*)d_in[7], (const u16*)d_in[8],
                                              (u16*)d_out, (unsigned char*)d_ws);
  dbg_print<<<1, 1, 0, stream>>>((unsigned char*)d_ws);
  for (int i = 0; i < n_in; ++i) fprintf(stderr, "PTR in%d %p\n", i, d_in[i]);
  fprintf(stderr, "PTR out %p ws %p size %zu\n", d_out, d_ws, ws_size);
}
```

```cpp
#include <hip/hip_runtime.h>

typedef unsigned short u16;
typedef unsigned u32;
typedef unsigned long long u64;
typedef u32 v4u __attribute__((ext_vector_type(4)));
typedef u32 v2u __attribute__((ext_vector_type(2)));
typedef float f32x4 __attribute__((ext_vector_type(4)));
typedef short s16x4 __attribute__((ext_vector_type(4)));
typedef __bf16 bf16x8 __attribute__((ext_vector_type(8)));
typedef __bf16 bf16x2 __attribute__((ext_vector_type(2)));
#define LAS __attribute__((address_space(3)))
#define GAS __attribute__((address_space(1)))
typedef __amdgpu_buffer_rsrc_t rsrc_t;

namespace {
constexpr int kD = 512, kSK = 4096, kW0 = 3072, kF = 1792;
constexpr int RK = 0, RV = 65536, RWQ = RV + 49152, RA = 131072, RD = 147456, RM = 155648;
constexpr int XN = RM, OL = RM, FR = RM + 1024, QL = RM + 2048, PL = RM + 3328, MLM = RM + 5376, MLL = RM + 5504,
              HL = RM + 5632, FL = RM + 6144;
constexpr int F_LP = FL, F_XN = FL + 4, F_WQ = FL + 8, F_GU = FL + 12, F_Q = FL + 16, F_D = FL + 20, F_P = FL + 24, F_H = FL + 28;
constexpr int EL = FL + 64, ST = FL + 320, RX = RD;
constexpr int LDS_BYTES = 163840;
constexpr int WS_E = 0, WS_QP = 4096, WS_HP = WS_QP + 4 * 8 * 2048 * 8, WS_OP = WS_HP + 4 * 2 * 8 * 896 * 8,
              WS_ML = WS_OP + 8 * 32 * 4 * 64 * 8, WS_SP = WS_ML + 8 * 32 * 4 * 16, WS_DP = WS_SP + 64 * 8 * 512 * 8,
              WS_END = WS_DP + 56 * 8 * 512 * 8;
constexpr int OOB = 0x7ffffff0;
constexpr int AUX_NT = 2, AUX_SC1 = 16, RSRC3 = 0x00020000;
constexpr u32 kSpinLds = 1u << 22, kSpinGlb = 1u << 16;

__device__ __forceinline__ float bflo(u32 w) { return __builtin_bit_cast(float, w << 16); }
__device__ __forceinline__ float bfhi(u32 w) { return __builtin_bit_cast(float, w & 0xffff0000u); }
__device__ __forceinline__ u32 pk(float lo, float hi) {
  bf16x2 v = {(__bf16)lo, (__bf16)hi};
  return __builtin_bit_cast(u32, v);
}
__device__ __forceinline__ float rbf(float f) { return (float)(__bf16)f; }
__device__ __forceinline__ f32x4 mfma(v4u a, v4u b, f32x4 c) {
  return __builtin_amdgcn_mfma_f32_16x16x32_bf16(__builtin_bit_cast(bf16x8, a), __builtin_bit_cast(bf16x8, b), c, 0, 0, 0);
}
template <int CTRL> __device__ __forceinline__ float dpp(float x) {
  return __builtin_bit_cast(float, __builtin_amdgcn_mov_dpp(__builtin_bit_cast(int, x), CTRL, 0xf, 0xf, true));
}
constexpr int XOR1 = 0xB1, XOR2 = 0x4E, XOR7 = 0x141, XOR8 = 0x128, ROR4 = 0x124;
__device__ __forceinline__ float x16_sum(float x) {
  auto s = __builtin_amdgcn_permlane16_swap(__float_as_uint(x), __float_as_uint(x), false, false);
  return __uint_as_float(s[0]) + __uint_as_float(s[1]);
}
__device__ __forceinline__ float x32_sum(float x) {
  auto s = __builtin_amdgcn_permlane32_swap(__float_as_uint(x), __float_as_uint(x), false, false);
  return __uint_as_float(s[0]) + __uint_as_float(s[1]);
}
__device__ __forceinline__ float x16_max(float x) {
  auto s = __builtin_amdgcn_permlane16_swap(__float_as_uint(x), __float_as_uint(x), false, false);
  return fmaxf(__uint_as_float(s[0]), __uint_as_float(s[1]));
}
__device__ __forceinline__ float x32_max(float x) {
  auto s = __builtin_amdgcn_permlane32_swap(__float_as_uint(x), __float_as_uint(x), false, false);
  return fmaxf(__uint_as_float(s[0]), __uint_as_float(s[1]));
}
__device__ __forceinline__ float wave_sum(float x) {
  x += dpp<XOR1>(x); x += dpp<XOR2>(x); x += dpp<XOR7>(x); x += dpp<XOR8>(x);
  return x32_sum(x16_sum(x));
}
__device__ __forceinline__ float silu(float x) { return x * __builtin_amdgcn_rcpf(1.f + __builtin_amdgcn_exp2f(x * -1.4426950408889634f)); }

__device__ __forceinline__ void lds_set(int addr, u32 val) {
  asm volatile("ds_write_b32 %0, %1" ::"v"(addr), "v"(val) : "memory");
}
__device__ __forceinline__ u32 lds_get(int addr) {
  u32 r;
  asm volatile("ds_read_b32 %0, %1\n\ts_waitcnt lgkmcnt(0)" : "=v"(r) : "v"(addr) : "memory");
  return __builtin_amdgcn_readfirstlane(r);
}
__device__ __forceinline__ u32 lds_wait_ge(int addr, u32 want) {
  u32 v = lds_get(addr);
  for (u32 n = 0; v < want && n < kSpinLds; ++n) { __builtin_amdgcn_s_sleep(1); v = lds_get(addr); }
  return v;
}
__device__ __forceinline__ u32 lds_spin_ge(int addr, u32 want) {
  u32 v = lds_get(addr);
  for (u32 n = 0; v < want && n < kSpinLds; ++n) v = lds_get(addr);
  return v;
}
__device__ __forceinline__ void lds_arrive(int addr, int) {
  asm volatile("s_mov_b64 exec, 1\n\tds_add_u32 %0, %1\n\ts_mov_b64 exec, -1" ::"v"(addr), "v"(1u) : "memory");
}
__device__ __forceinline__ v4u ws_ld(rsrc_t rs, int off) { return __builtin_amdgcn_raw_buffer_load_b128(rs, off, 0, AUX_SC1); }
__device__ __forceinline__ void ws_st(rsrc_t rs, int off, v4u v) { __builtin_amdgcn_raw_buffer_store_b128(v, rs, off, 0, AUX_SC1); }
__device__ __forceinline__ bool tag_ok(v4u u, u32 tag) { return u.y == tag && u.w == tag; }
__device__ __forceinline__ bool all_of(bool ok) { return __builtin_amdgcn_ballot_w64(!ok) == 0ull; }
typedef int s4i __attribute__((ext_vector_type(4)));
#define POLL_REGS "v96", "v97", "v98", "v99", "v100", "v101", "v102", "v103", "v104", "v105", "v106", "v107"
__device__ __forceinline__ void poll_a(s4i rs, int off) {
  asm volatile("buffer_load_dwordx4 v[96:99], %0, %1, 0 offen sc1" ::"v"(off), "s"(rs) : "memory", POLL_REGS);
}
__device__ __forceinline__ void poll_b(s4i rs, int off) {
  asm volatile("buffer_load_dwordx4 v[100:103], %0, %1, 0 offen sc1" ::"v"(off), "s"(rs) : "memory", POLL_REGS);
}
__device__ __forceinline__ void poll_c(s4i rs, int off) {
  asm volatile("buffer_load_dwordx4 v[104:107], %0, %1, 0 offen sc1" ::"v"(off), "s"(rs) : "memory", POLL_REGS);
}
__device__ __forceinline__ v4u take_c() {
  v4u u;
  asm volatile("s_waitcnt vmcnt(2)\n\tv_mov_b32 %0, v104\n\tv_mov_b32 %1, v105\n\tv_mov_b32 %2, v106\n\tv_mov_b32 %3, v107"
               : "=v"(u.x), "=v"(u.y), "=v"(u.z), "=v"(u.w)::"memory", POLL_REGS);
  return u;
}
__device__ __forceinline__ v2u poll_end(s4i rs, int off, u32 tag, u32& left) {
  u32 x, z, cnt;
  int ad;
  u64 ok, t;
#define POLL_STEP(r0, r1, r2, r3)                                                                   \
  "s_waitcnt vmcnt(1)\n\t"                                                                          \
  "v_cmp_eq_u32_e32 vcc, %[tag], v" #r1 "\n\t"                                                      \
  "v_cmp_eq_u32_e64 %[t], %[tag], v" #r3 "\n\t"                                                     \
  "s_and_b64 vcc, vcc, %[t]\n\t"                                                                    \
  "s_andn2_b64 vcc, vcc, %[ok]\n\t"                                                                 \
  "v_cndmask_b32_e32 %[x], %[x], v" #r0 ", vcc\n\t"                                                 \
  "v_cndmask_b32_e32 %[z], %[z], v" #r2 ", vcc\n\t"                                                 \
  "s_or_b64 %[ok], %[ok], vcc\n\t"                                                                  \
  "s_cmp_eq_u64 %[ok], -1\n\t"                                                                      \
  "s_cbranch_scc1 .Lpoll_done_%=\n\t"                                                               \
  "v_cndmask_b32_e64 %[ad], %[off], %[oob], %[ok]\n\t"                                              \
  "buffer_load_dwordx4 v[" #r0 ":" #r3 "], %[ad], %[rs], 0 offen sc1\n\t"
  asm volatile("s_mov_b64 %[ok], 0\n\t"
               "s_movk_i32 %[cnt], 0x1000\n"
               ".Lpoll_loop_%=:\n\t" POLL_STEP(96, 97, 98, 99) POLL_STEP(100, 101, 102, 103)
               "s_sub_u32 %[cnt], %[cnt], 1\n\t"
               "s_cmp_lg_u32 %[cnt], 0\n\t"
               "s_cbranch_scc1 .Lpoll_loop_%=\n"
               ".Lpoll_done_%=:\n\t"
               "s_nop 0"
               : [x] "=&v"(x), [z] "=&v"(z), [ad] "=&v"(ad), [ok] "=&s"(ok), [t] "=&s"(t), [cnt] "=&s"(cnt)
               : [off] "v"(off), [oob] "v"(OOB), [rs] "s"(rs), [tag] "s"(tag)
               : "memory", "vcc", "scc", POLL_REGS);
  left = cnt;
  return v2u{x, z};
}
__device__ __forceinline__ v2u ws_take(s4i rs, int off, v4u u, u32 tag, u32& n) {
  n = 0x1001;
  if (all_of(tag_ok(u, tag))) return v2u{u.x, u.z};
  poll_a(rs, off);
  __builtin_amdgcn_s_sleep(4);
  poll_b(rs, off);
  return poll_end(rs, off, tag, n);
}
__device__ __forceinline__ u32 now() { return (u32)__builtin_amdgcn_s_memrealtime(); }
__device__ __forceinline__ void wait_until(u32 t0, u32 dt) {
  while (now() - t0 < dt) __builtin_amdgcn_s_sleep(1);
}
__device__ __forceinline__ v4u tr_frag(LAS unsigned char* p, int half) {
  const v2u a = __builtin_bit_cast(v2u, __builtin_amdgcn_ds_read_tr16_b64_v4i16((LAS s16x4*)p));
  const v2u b = __builtin_bit_cast(v2u, __builtin_amdgcn_ds_read_tr16_b64_v4i16((LAS s16x4*)(p + half)));
  return v4u{a.x, a.y, b.x, b.y};
}
}

#define LAG 5
#define LAGK 5
#define SPDELAY 8
#define TQ1 350
#define TQ2 430
#define ODELAY 8
#define PGAP 3
#define QPOLL 6
#define HPOLL 9
#define STAMP(i)
#define STAMPV(i, v)
#define STAMP_DUMP()

__global__ void __launch_bounds__(640, 3)
fused_decode_block(const u16* __restrict__ x, const u16* __restrict__ kc, const u16* __restrict__ vc, const u16* __restrict__ wq,
                   const u16* __restrict__ wo, const u16* __restrict__ wg, const u16* __restrict__ wu, const u16* __restrict__ wd,
                   u16* __restrict__ out, unsigned char* __restrict__ ws) {
  __shared__ __attribute__((aligned(16))) unsigned char lds[LDS_BYTES];
  LAS unsigned char* L = (LAS unsigned char*)lds;
  const int tid = threadIdx.x, lane = tid & 63, wave = __builtin_amdgcn_readfirstlane(tid >> 6);
  asm volatile("" ::"s"(x), "s"(kc), "s"(vc), "s"(wq), "s"(wo), "s"(wg), "s"(wu), "s"(wd), "s"(out), "s"(ws));
  const u32 t0 = now();
  const int c = blockIdx.x, g8 = c & 7, r = c >> 3;
  const int fb = c >> 5, fc = (c & 31) * 16;
  const int j = c >> 6, t64 = c & 63;
  const bool has_gu = t64 < 56;
  const int fg = (r >> 2) * 8 + g8, cg = r & 3;
  const bool has_dn = fg < 56;
  const int dt = r >> 2;
  const int ab = r >> 2, as = r & 3;
  const rsrc_t rw = __builtin_amdgcn_make_buffer_rsrc((void*)ws, 0, WS_END, RSRC3);
  const s4i rp = {(int)(u32)(u64)ws, (int)((u32)((u64)ws >> 32) & 0xffffu), WS_END, RSRC3};
  const u32 TH_K = has_gu ? 5 : 3, TH_V = TH_K + 8, TH_DN = TH_V + 9, TH_WO = TH_V + 8 + (has_dn ? 1 : 0) + 2;
  STAMP(0);

  if (__builtin_expect(wave == 0, 1)) {
    const int r8 = lane >> 3, r4 = lane >> 4;
    const int sw128 = ((lane & 7) ^ ((r4 & 3) << 1)) * 16;
    const int swV = ((lane & 15) ^ (r4 << 1)) * 16;
    const int swK = ((lane & 15) ^ r4) * 16;
    u32 cnt, lpv = 1;
    __builtin_amdgcn_s_setprio(3);
#define GLDS(rs, ldsoff, voff, soff) __builtin_amdgcn_raw_ptr_buffer_load_lds(rs, (LAS void*)(L + (ldsoff)), 16, voff, soff, 0, AUX_NT)
#define GROUP_DONE_L(LG) do { asm volatile("s_waitcnt vmcnt(%0)" ::"n"((LG) * 8) : "memory"); ++cnt; if (cnt + 1 > lpv + (LG)) { lpv = cnt + 1 - (LG); lds_set(F_LP, lpv); } } while (0)
#define GROUP_DONE() GROUP_DONE_L(LAG)
    {
      const rsrc_t rs = __builtin_amdgcn_make_buffer_rsrc((void*)x, 0, 8192, RSRC3);
#pragma unroll
      for (int i = 0; i < 8; ++i) __builtin_amdgcn_raw_ptr_buffer_load_lds(rs, (LAS void*)(L + RX + i * 1024), 16, lane * 16, i * 1024, 0, 0);
      __builtin_amdgcn_raw_ptr_buffer_load_lds(rw, (LAS void*)(L + EL), 4, lane * 4, WS_E, 0, AUX_SC1);
    }
    {
      const rsrc_t rs = __builtin_amdgcn_make_buffer_rsrc((void*)wq, 0, 512 * 4096 * 2, RSRC3);
      const int vo = r8 * 8192 + sw128, so = j * 128 * 8192 + t64 * 128;
#pragma unroll
      for (int i = 0; i < 16; ++i) GLDS(rs, RWQ + i * 1024, vo, so + i * 8 * 8192);
    }
    STAMP(1);
    if (has_gu) {
      const rsrc_t rs = __builtin_amdgcn_make_buffer_rsrc((void*)(t64 < 28 ? wg : wu), 0, 512 * kF * 2, RSRC3);
      const int vo = r8 * (kF * 2) + sw128, so = j * 128 * (kF * 2) + (t64 < 28 ? t64 : t64 - 28) * 128;
#pragma unroll
      for (int i = 0; i < 16; ++i) GLDS(rs, RA + i * 1024, vo, so + i * 8 * (kF * 2));
      __builtin_amdgcn_s_barrier();
      asm volatile("s_waitcnt vmcnt(32)" ::: "memory");
      cnt = 4;
    } else {
      __builtin_amdgcn_s_barrier();
      asm volatile("s_waitcnt vmcnt(16)" ::: "memory");
      cnt = 2;
    }
    lds_set(F_LP, 1u);
    u32 xfin;
    asm volatile("ds_read_b32 %0, %1" : "=v"(xfin) : "v"(RX + (fb * kD + fc + (lane & 7) * 2) * 2) : "memory");
    const u32 tag = lds_get(EL) + 1u;
    asm volatile("" : "+v"(xfin));
    STAMP(2);
    const size_t kvoff = ((size_t)(ab * 8 + g8) * kSK + kW0 + as * 256) * 128;
    {
      const rsrc_t rs = __builtin_amdgcn_make_buffer_rsrc((void*)(kc + kvoff), 0, 65536, RSRC3);
      const int vo = r4 * 256;
#pragma unroll 1
      for (int n = 0; n < 8; ++n) {
#pragma unroll
        for (int i = 0; i < 8; ++i) GLDS(rs, RK + n * 8192 + i * 1024, vo + (swK ^ ((i & 3) * 64)), n * 8192 + i * 1024);
        GROUP_DONE_L(LAGK);
      }
    }
    STAMP(3);
    {
      const rsrc_t rs = __builtin_amdgcn_make_buffer_rsrc((void*)(vc + kvoff), 0, 65536, RSRC3);
      const int vo = r4 * 256;
#pragma unroll 1
      for (int n = 0; n < 8; ++n) {
        if (n == 6) lds_wait_ge(F_WQ, 4);
#pragma unroll
        for (int i = 0; i < 8; ++i) GLDS(rs, RV + n * 8192 + i * 1024, vo + (swV ^ ((i & 1) * 128)), n * 8192 + i * 1024);
        GROUP_DONE();
      }
    }
    STAMP(4);
    if (has_dn) {
      const rsrc_t rs = __builtin_amdgcn_make_buffer_rsrc((void*)wd, 0, kF * 512 * 2, RSRC3);
      const int vo = r4 * 1024, so = fg * 32 * 1024 + cg * 256;
#pragma unroll
      for (int i = 0; i < 8; ++i) GLDS(rs, RD + i * 1024, vo + (swV ^ ((i & 1) * 128)), so + i * 4096);
      GROUP_DONE();
    }
    if (has_gu) lds_wait_ge(F_GU, 4);
    {
      const rsrc_t rs = __builtin_amdgcn_make_buffer_rsrc((void*)wo, 0, 4096 * 512 * 2, RSRC3);
      const int vo = r4 * 1024, so = (g8 * 512 + dt * 16) * 1024 + cg * 256;
#pragma unroll
      for (int n = 0; n < 2; ++n) {
#pragma unroll
        for (int i = 0; i < 8; ++i)
          GLDS(rs, RA + n * 8192 + i * 1024, vo + (swV ^ ((i & 1) * 128)), so + ((2 * n + (i >> 2)) * 128 + (i & 3) * 4) * 1024);
        GROUP_DONE();
      }
    }
    STAMP(5);
    asm volatile("s_waitcnt vmcnt(32)" ::: "memory"); lds_set(F_LP, cnt - 3);
    asm volatile("s_waitcnt vmcnt(24)" ::: "memory"); lds_set(F_LP, cnt - 2);
    STAMP(6);
    asm volatile("s_waitcnt vmcnt(16)" ::: "memory"); lds_set(F_LP, cnt - 1);
    asm volatile("s_waitcnt vmcnt(8)" ::: "memory");  lds_set(F_LP, cnt);
    asm volatile("s_waitcnt vmcnt(0)" ::: "memory");  lds_set(F_LP, cnt + 1);
    STAMP(7);
    __builtin_amdgcn_s_barrier();
    float f0, f1;
    {
      const int doff = WS_DP + (((lane >> 3) * 8 + fb) * 512 + fc + (lane & 7) * 2) * 8;
      for (u32 n = 0; n < kSpinGlb; ++n) {
        asm volatile("" ::: "memory");
        v4u u[7];
#pragma unroll
        for (int i = 0; i < 7; ++i) u[i] = ws_ld(rw, doff + i * (8 * 8 * 512 * 8));
        bool ok = true;
        f0 = 0.f; f1 = 0.f;
#pragma unroll
        for (int i = 0; i < 7; ++i) { ok = ok && tag_ok(u[i], tag); f0 += __uint_as_float(u[i].x); f1 += __uint_as_float(u[i].z); }
        STAMPV(20, n);
        if (all_of(ok)) break;
      }
      f0 += dpp<XOR8>(f0); f1 += dpp<XOR8>(f1);
      f0 = rbf(silu(rbf(x32_sum(x16_sum(f0)))));
      f1 = rbf(silu(rbf(x32_sum(x16_sum(f1)))));
      asm volatile("" ::"v"(f0), "v"(f1));
    }
    STAMP(8);
    __builtin_amdgcn_s_barrier();
    asm volatile("" ::: "memory");
    LAS f32x4* fr = (LAS f32x4*)(L + FR + (lane & 7) * 64);
    const f32x4 a0 = fr[0], a1 = fr[1], a2 = fr[2], a3 = fr[3];
    const float s0 = rbf(((a0[0] + a0[1]) + (a0[2] + a0[3])) + ((a1[0] + a1[1]) + (a1[2] + a1[3])));
    const float s1 = rbf(((a2[0] + a2[1]) + (a2[2] + a2[3])) + ((a3[0] + a3[1]) + (a3[2] + a3[3])));
    { const u32 dur = now() - t0;
      u32 ov = pk(bflo(xfin) + rbf(s0 * f0), bfhi(xfin) + rbf(s1 * f1));
      if (dur > 1600) ov = pk((float)dur, (float)dur);
      if (lane < 8) ((GAS u32*)out)[(fb * kD + fc) / 2 + lane] = ov; }
    if (c == 0 && lane == 0) __builtin_amdgcn_raw_buffer_store_b32(tag, rw, WS_E, 0, AUX_SC1);
    STAMP(9);
    STAMP_DUMP();
    return;
  }

  const int cw = wave - 1, lc = lane & 15, lg = lane >> 4;
  if (cw == 0 && lane < 8) lds_set(FL + lane * 4, 0u);
  asm volatile("s_waitcnt lgkmcnt(0)" ::: "memory");
  __builtin_amdgcn_s_barrier();
  const int tq = lc >> 2, tp = lc & 3, row0 = 4 * lg + tq;
  if (wave == 9) {
    if (!has_dn) return;
    wait_until(t0, TQ2);
    lds_wait_ge(F_LP, 1);
    const u32 tag = lds_get(EL) + 1u;
    const int hoff = WS_HP + ((lane >> 3) * 896 + fg * 16 + (lane & 7) * 2) * 8;
    float g[4], u[4];
    for (u32 n = 0; n < kSpinGlb; ++n) {
      asm volatile("" ::: "memory");
      v4u t[8];
#pragma unroll
      for (int i = 0; i < 8; ++i) t[i] = ws_ld(rw, hoff + i * (8 * 896 * 8));
      bool ok = true;
#pragma unroll
      for (int i = 0; i < 8; ++i) ok = ok && tag_ok(t[i], tag);
      g[0] = (bflo(t[0].x) + bflo(t[2].x)) + (bflo(t[4].x) + bflo(t[6].x)); u[0] = (bflo(t[1].x) + bflo(t[3].x)) + (bflo(t[5].x) + bflo(t[7].x));
      g[1] = (bfhi(t[0].x) + bfhi(t[2].x)) + (bfhi(t[4].x) + bfhi(t[6].x)); u[1] = (bfhi(t[1].x) + bfhi(t[3].x)) + (bfhi(t[5].x) + bfhi(t[7].x));
      g[2] = (bflo(t[0].z) + bflo(t[2].z)) + (bflo(t[4].z) + bflo(t[6].z)); u[2] = (bflo(t[1].z) + bflo(t[3].z)) + (bflo(t[5].z) + bflo(t[7].z));
      g[3] = (bfhi(t[0].z) + bfhi(t[2].z)) + (bfhi(t[4].z) + bfhi(t[6].z)); u[3] = (bfhi(t[1].z) + bfhi(t[3].z)) + (bfhi(t[5].z) + bfhi(t[7].z));
      STAMPV(20, n);
      if (all_of(ok)) break;
    }
#pragma unroll
    for (int i = 0; i < 4; ++i) g[i] = rbf(silu(rbf(g[i]))) * rbf(u[i]);
    *(LAS v2u*)(L + HL + ((lane & 3) * 8 + (lane >> 3)) * 16 + ((lane >> 2) & 1) * 8) = v2u{pk(g[0], g[1]), pk(g[2], g[3])};
    const v4u hf = *(LAS v4u*)(L + HL + (lg * 8 + (lc & 7)) * 16);
    const int off = WS_DP + ((fg * 8 + (lc & 7)) * 512 + cg * 128 + lg * 4 + (lc >> 3) * 2) * 8;
    STAMP(1);
    lds_wait_ge(F_LP, TH_DN);
    STAMP(2);
#pragma unroll
    for (int i = 0; i < 8; ++i) {
      const f32x4 acc = mfma(tr_frag(L + RD + row0 * 256 + (((2 * i + (tp >> 1)) ^ ((row0 & 7) << 1)) * 16) + (tp & 1) * 8, 4096), hf,
                             f32x4{0.f, 0.f, 0.f, 0.f});
      ws_st(rw, off + i * 128, v4u{__float_as_uint(lc < 8 ? acc[0] : acc[2]), tag, __float_as_uint(lc < 8 ? acc[1] : acc[3]), tag});
    }
    STAMP(3);
    STAMP_DUMP();
    return;
  }
  u32 lp = lds_spin_ge(F_LP, 1);
  STAMP(1);
  const u32 tag = lds_get(EL) + 1u;
  {
    const v4u xr = *(LAS v4u*)(L + RX + cw * 1024 + lane * 16);
    float v[8];
#pragma unroll
    for (int i = 0; i < 4; ++i) { v[2 * i] = bflo(xr[i]); v[2 * i + 1] = bfhi(xr[i]); }
    const float x0 = __builtin_bit_cast(float, __builtin_amdgcn_readfirstlane(xr[0] << 16));
    float s0 = 0.f, q0 = 0.f;
#pragma unroll
    for (int i = 0; i < 8; ++i) { const float d = v[i] - x0; s0 += d; q0 = fmaf(d, d, q0); }
    s0 = wave_sum(s0) * (1.f / kD);
    q0 = wave_sum(q0) * (1.f / kD);
    const float mb = rbf(x0 + s0), vb = rbf(fmaxf(fmaf(-s0, s0, q0), 0.f));
    const float rden = __builtin_amdgcn_rcpf(rbf(__builtin_amdgcn_sqrtf(rbf(vb + 1e-5f))));
#pragma unroll
    for (int i = 0; i < 8; ++i) v[i] = rbf(v[i] - mb) * rden;
    if (lg == j) {
      const int ks = lc >> 2, o = lc & 3, gq = (o & 1) * 2, hf = o >> 1;
      LAS v2u* p = (LAS v2u*)(L + XN + ((ks * 4 + gq) * 8 + cw) * 16 + hf * 8);
      p[0] = v2u{pk(v[0], v[1]), pk(v[2], v[3])};
      p[16] = v2u{pk(v[4], v[5]), pk(v[6], v[7])};
    }
    lds_arrive(F_XN, lane);
  }
  STAMP(2);
  if (cw < 4 || has_gu) {
    const int nt = cw & 3;
    lds_spin_ge(F_XN, 8);
    v4u xf[4];
#pragma unroll
    for (int ks = 0; ks < 4; ++ks) xf[ks] = *(LAS v4u*)(L + XN + ((ks * 4 + lg) * 8 + (lc & 7)) * 16);
    lp = lds_spin_ge(F_LP, cw < 4 ? 3u : 5u);
    STAMP(3);
    LAS unsigned char* base = L + (cw < 4 ? RWQ : RA) + row0 * 128 + (((2 * nt + (tp >> 1)) ^ (((row0 >> 1) & 3) << 1)) * 16) + (tp & 1) * 8;
    f32x4 acc = {0.f, 0.f, 0.f, 0.f};
#pragma unroll
    for (int ks = 0; ks < 4; ++ks) acc = mfma(tr_frag(base + ks * 4096, 2048), xf[ks], acc);
    if (lc < 8) {
      const v4u o = {pk(acc[0], acc[1]), tag, pk(acc[2], acc[3]), tag};
      const int off = cw < 4 ? WS_QP + ((j * 8 + lc) * 2048 + t64 * 32 + nt * 8 + lg * 2) * 8
                             : WS_HP + (((j * 2 + (t64 >= 28)) * 8 + lc) * 896 + (t64 >= 28 ? t64 - 28 : t64) * 32 + nt * 8 + lg * 2) * 8;
      ws_st(rw, off, o);
    }
    lds_arrive(cw < 4 ? F_WQ : F_GU, lane);
    STAMP(4);
  }
  u32 n;
  {
    const int qoff = WS_QP + ((lg * 8 + ab) * 2048 + g8 * 256 + (cw * 16 + lc) * 2) * 8;
    wait_until(t0, TQ1);
    STAMP(5);
    const v4u uq4 = ws_ld(rw, qoff);
    const v2u uq = ws_take(rp, qoff, uq4, tag, n);
    STAMPV(20, 0x1001 - n);
    STAMP(6);
    const float q0 = x32_sum(x16_sum(bflo(uq.x))), q1 = x32_sum(x16_sum(bfhi(uq.x))), q2 = x32_sum(x16_sum(bflo(uq.y))),
                q3 = x32_sum(x16_sum(bfhi(uq.y)));
    if (lg == 0) {
      const int d = cw * 64 + lc * 4;
      *(LAS v2u*)(L + QL + (d >> 7) * 272 + (d & 127) * 2) = v2u{pk(q0, q1), pk(q2, q3)};
    }
    lds_arrive(F_Q, lane);
  }
  {
    if (lp < TH_K + cw + 1) lp = lds_wait_ge(F_LP, TH_K + cw + 1);
    v4u kf[8];
#pragma unroll
    for (int i = 0; i < 8; ++i) kf[i] = *(LAS v4u*)(L + RK + (cw * 32 + (i >> 2) * 16 + lc) * 256 + ((((i & 3) * 4 + lg) ^ lc) * 16));
    lds_spin_ge(F_Q, 8);
    STAMP(7);
    v4u qf[4];
#pragma unroll
    for (int ks = 0; ks < 4; ++ks) qf[ks] = *(LAS v4u*)(L + QL + (lc & 3) * 272 + ks * 64 + lg * 16);
    f32x4 a0 = {0.f, 0.f, 0.f, 0.f}, a1 = {0.f, 0.f, 0.f, 0.f};
#pragma unroll
    for (int ks = 0; ks < 4; ++ks) { a0 = mfma(kf[ks], qf[ks], a0); a1 = mfma(kf[4 + ks], qf[ks], a1); }
    float sc[8];
#pragma unroll
    for (int i = 0; i < 4; ++i) { sc[i] = rbf(rbf(a0[i]) * 0.08838834764831845f); sc[4 + i] = rbf(rbf(a1[i]) * 0.08838834764831845f); }
    float m = fmaxf(fmaxf(fmaxf(sc[0], sc[1]), fmaxf(sc[2], sc[3])), fmaxf(fmaxf(sc[4], sc[5]), fmaxf(sc[6], sc[7])));
    m = x32_max(x16_max(m));
#pragma unroll
    for (int i = 0; i < 8; ++i) sc[i] = __builtin_amdgcn_exp2f((sc[i] - m) * 1.4426950408889634f);
    if (lc < 4) *(LAS v4u*)(L + PL + ((cw * 4 + lg) * 4 + lc) * 16) = v4u{pk(sc[0], sc[1]), pk(sc[2], sc[3]), pk(sc[4], sc[5]), pk(sc[6], sc[7])};
    const float l = x32_sum(x16_sum(((sc[0] + sc[1]) + (sc[2] + sc[3])) + ((sc[4] + sc[5]) + (sc[6] + sc[7]))));
    if (lane < 4) { *(LAS float*)(L + MLM + (lane * 8 + cw) * 4) = m; *(LAS float*)(L + MLL + (lane * 8 + cw) * 4) = l; }
    lds_arrive(F_P, lane);
  }
  lds_spin_ge(F_P, 8);
  STAMP(8);
  const int wb = row0 * 256 + (((2 * cw + (tp >> 1)) ^ ((row0 & 7) << 1)) * 16) + (tp & 1) * 8;
  int opw = WS_OP + ((((g8 * 8 + cw) * 8 + ab) * 4 + as) * 16 + lc * 4 + lg) * 16;
  int ooff = WS_OP + ((((g8 * 8 + dt) * 8 + cw) * 4 + lg) * 16 + lc) * 16;
  int moff = WS_ML + (((cw * 8 + g8) * 4 + lg) * 4 + (lc >> 2)) * 16;
  int spw = WS_SP + (((g8 * 8 + dt) * 8 + (lc & 7)) * 512 + cg * 128 + cw * 16 + lg * 4 + (lc >> 3) * 2) * 8;
  int soff = WS_SP + (((cw * 8 + (lane >> 3)) * 8 + fb) * 512 + fc + (lane & 7) * 2) * 8;
  int olw = OL + (((lc >> 3) * 4 + (lc & 3)) * 8 + cw) * 16 + ((lc >> 2) & 1) * 8, olr = OL + (lg * 8 + (lc & 7)) * 16;
  int frw = FR + (lane & 7) * 64 + cw * 4;
  {
    const f32x4 m0 = *(LAS f32x4*)(L + MLM + (lc & 3) * 32), m1 = *(LAS f32x4*)(L + MLM + (lc & 3) * 32 + 16);
    v4u pf[8];
#pragma unroll
    for (int i = 0; i < 8; ++i) pf[i] = *(LAS v4u*)(L + PL + ((i * 4 + lg) * 4 + (lc & 3)) * 16);
    const float M = fmaxf(fmaxf(fmaxf(m0[0], m0[1]), fmaxf(m0[2], m0[3])), fmaxf(fmaxf(m1[0], m1[1]), fmaxf(m1[2], m1[3])));
    float f[8];
#pragma unroll
    for (int i = 0; i < 4; ++i) {
      f[i] = __builtin_amdgcn_exp2f((m0[i] - M) * 1.4426950408889634f);
      f[4 + i] = __builtin_amdgcn_exp2f((m1[i] - M) * 1.4426950408889634f);
    }
    if (cw == 0 && lane < 4) {
      const f32x4 l0 = *(LAS f32x4*)(L + MLL + lane * 32), l1 = *(LAS f32x4*)(L + MLL + lane * 32 + 16);
      const float l = (fmaf(l0[0], f[0], l0[1] * f[1]) + fmaf(l0[2], f[2], l0[3] * f[3])) + (fmaf(l1[0], f[4], l1[1] * f[5]) + fmaf(l1[2], f[6], l1[3] * f[7]));
      ws_st(rw, WS_ML + (((ab * 8 + g8) * 4 + as) * 4 + lane) * 16, v4u{__float_as_uint(M), tag, __float_as_uint(l), tag});
    }
    const f32x4 z = {0.f, 0.f, 0.f, 0.f};
    f32x4 acc = z;
    asm volatile("" : "+v"(ooff), "+v"(moff), "+v"(soff), "+v"(spw), "+v"(opw), "+v"(olw), "+v"(olr), "+v"(frw));
    if (lp < TH_V + 5) lp = lds_wait_ge(F_LP, TH_V + 5);
#pragma unroll
    for (int i = 0; i < 5; ++i) acc += f[i] * mfma(tr_frag(L + RV + wb + i * 8192, 4096), pf[i], z);
#pragma unroll
    for (int i = 5; i < 8; ++i) {
      if (lp < TH_V + i + 1) lp = lds_spin_ge(F_LP, TH_V + i + 1);
      acc += f[i] * mfma(tr_frag(L + RV + wb + i * 8192, 4096), pf[i], z);
    }
    if (lc < 4)
      ws_st(rw, opw, v4u{pk(acc[0], acc[1]), tag, pk(acc[2], acc[3]), tag});
    poll_c(rp, moff);
    STAMP(9);
  }
  {
    __builtin_amdgcn_s_sleep(ODELAY);
    STAMP(10);
    poll_a(rp, ooff);
    __builtin_amdgcn_s_sleep(PGAP);
    poll_b(rp, ooff);
    v4u um = take_c();
    if (__builtin_expect(!all_of(tag_ok(um, tag)), 0)) {
      n = 0;
      do {
        asm volatile("" ::: "memory");
        um = ws_ld(rw, moff);
      } while (!all_of(tag_ok(um, tag)) && ++n < kSpinGlb);
      STAMPV(25, n + 1);
    }
    const float ms = __uint_as_float(um.x), Mx = x32_max(x16_max(ms));
    const float w = __builtin_amdgcn_exp2f((ms - Mx) * 1.4426950408889634f);
    const float sc2 = w * __builtin_amdgcn_rcpf(x32_sum(x16_sum(w * __uint_as_float(um.z))));
    lds_spin_ge(F_LP, TH_WO);
    const v4u wf0 = tr_frag(L + RA + wb, 4096), wf1 = tr_frag(L + RA + wb + 8192, 4096);
    asm volatile("" ::"v"(sc2), "v"(wf0), "v"(wf1));
    const v2u uo = poll_end(rp, ooff, tag, n);
    STAMPV(22, 0x1000 - n);
    STAMP(11);
    const float o0 = x32_sum(x16_sum(sc2 * bflo(uo.x))), o1 = x32_sum(x16_sum(sc2 * bfhi(uo.x))),
                o2 = x32_sum(x16_sum(sc2 * bflo(uo.y))), o3 = x32_sum(x16_sum(sc2 * bfhi(uo.y)));
    if (lg == 0)
      *(LAS v2u*)(L + olw) = v2u{pk(o0, o1), pk(o2, o3)};
    asm volatile("s_waitcnt lgkmcnt(0)" ::: "memory");
    __builtin_amdgcn_s_barrier();
    asm volatile("" ::: "memory");
    f32x4 acc = mfma(wf0, *(LAS v4u*)(L + olr), f32x4{0.f, 0.f, 0.f, 0.f});
    acc = mfma(wf1, *(LAS v4u*)(L + olr + 512), acc);
    ws_st(rw, spw, v4u{__float_as_uint(lc < 8 ? acc[0] : acc[2]), tag, __float_as_uint(lc < 8 ? acc[1] : acc[3]), tag});
  }
  STAMP(12);
  {
    __builtin_amdgcn_s_sleep(SPDELAY);
    STAMP(13);
    poll_a(rp, soff);
    __builtin_amdgcn_s_sleep(PGAP);
    poll_b(rp, soff);
    const v2u us = poll_end(rp, soff, tag, n);
    STAMPV(23, 0x1000 - n);
    STAMP(14);
    float s0 = __uint_as_float(us.x), s1 = __uint_as_float(us.y);
    s0 += dpp<XOR8>(s0); s1 += dpp<XOR8>(s1);
    s0 = x32_sum(x16_sum(s0)); s1 = x32_sum(x16_sum(s1));
    if (lane < 8) { *(LAS float*)(L + frw) = s0; *(LAS float*)(L + frw + 32) = s1; }
    asm volatile("s_waitcnt lgkmcnt(0)" ::: "memory");
    __builtin_amdgcn_s_barrier();
    STAMP(15);
    STAMP_DUMP();
  }
}

extern "C" void kernel_launch(void* const* d_in, const int* in_sizes, int n_in, void* d_out, int out_size, void* d_ws,
                              size_t ws_size, hipStream_t stream) {
  fused_decode_block<<<256, 640, 0, stream>>>((const u16*)d_in[0], (const u16*)d_in[1], (const u16*)d_in[2], (const u16*)d_in[3],
                                              (const u16*)d_in[4], (const u16*)d_in[6], (const u16*)d_in[7], (const u16*)d_in[8],
                                              (u16*)d_out, (unsigned char*)d_ws);
}
```

```cpp
#include <hip/hip_runtime.h>

typedef unsigned short u16;
typedef unsigned u32;
typedef unsigned long long u64;
typedef u32 v4u __attribute__((ext_vector_type(4)));
typedef u32 v2u __attribute__((ext_vector_type(2)));
typedef float f32x4 __attribute__((ext_vector_type(4)));
typedef short s16x4 __attribute__((ext_vector_type(4)));
typedef __bf16 bf16x8 __attribute__((ext_vector_type(8)));
typedef __bf16 bf16x2 __attribute__((ext_vector_type(2)));
#define LAS __attribute__((address_space(3)))
#define GAS __attribute__((address_space(1)))
typedef __amdgpu_buffer_rsrc_t rsrc_t;

namespace {
constexpr int kD = 512, kSK = 4096, kW0 = 3072, kF = 1792;
constexpr int RK = 0, RV = 65536, RWQ = RV + 49152, RA = 131072, RD = 147456, RM = 155648;
constexpr int XN = RM, OL = RM, FR = RM + 1024, QL = RM + 2048, PL = RM + 3328, MLM = RM + 5376, MLL = RM + 5504,
              HL = RM + 5632, FL = RM + 6144;
constexpr int F_LP = FL, F_XN = FL + 4, F_WQ = FL + 8, F_GU = FL + 12, F_Q = FL + 16, F_D = FL + 20, F_P = FL + 24, F_H = FL + 28;
constexpr int EL = FL + 64, ST = FL + 320, RX = RD;
constexpr int LDS_BYTES = 163840;
constexpr int WS_E = 0, WS_QP = 4096, WS_HP = WS_QP + 4 * 8 * 2048 * 8, WS_OP = WS_HP + 4 * 2 * 8 * 896 * 8,
              WS_ML = WS_OP + 8 * 32 * 4 * 64 * 8, WS_SP = WS_ML + 8 * 32 * 4 * 16, WS_DP = WS_SP + 64 * 8 * 512 * 8,
              WS_END = WS_DP + 56 * 8 * 512 * 8;
constexpr int OOB = 0x7ffffff0;
constexpr int AUX_NT = 2, AUX_SC1 = 16, RSRC3 = 0x00020000;
constexpr u32 kSpinLds = 1u << 22, kSpinGlb = 1u << 16;

__device__ __forceinline__ float bflo(u32 w) { return __builtin_bit_cast(float, w << 16); }
__device__ __forceinline__ float bfhi(u32 w) { return __builtin_bit_cast(float, w & 0xffff0000u); }
__device__ __forceinline__ u32 pk(float lo, float hi) {
  bf16x2 v = {(__bf16)lo, (__bf16)hi};
  return __builtin_bit_cast(u32, v);
}
__device__ __forceinline__ float rbf(float f) { return (float)(__bf16)f; }
__device__ __forceinline__ f32x4 mfma(v4u a, v4u b, f32x4 c) {
  return __builtin_amdgcn_mfma_f32_16x16x32_bf16(__builtin_bit_cast(bf16x8, a), __builtin_bit_cast(bf16x8, b), c, 0, 0, 0);
}
template <int CTRL> __device__ __forceinline__ float dpp(float x) {
  return __builtin_bit_cast(float, __builtin_amdgcn_mov_dpp(__builtin_bit_cast(int, x), CTRL, 0xf, 0xf, true));
}
constexpr int XOR1 = 0xB1, XOR2 = 0x4E, XOR7 = 0x141, XOR8 = 0x128, ROR4 = 0x124;
__device__ __forceinline__ float x16_sum(float x) {
  auto s = __builtin_amdgcn_permlane16_swap(__float_as_uint(x), __float_as_uint(x), false, false);
  return __uint_as_float(s[0]) + __uint_as_float(s[1]);
}
__device__ __forceinline__ float x32_sum(float x) {
  auto s = __builtin_amdgcn_permlane32_swap(__float_as_uint(x), __float_as_uint(x), false, false);
  return __uint_as_float(s[0]) + __uint_as_float(s[1]);
}
__device__ __forceinline__ float x16_max(float x) {
  auto s = __builtin_amdgcn_permlane16_swap(__float_as_uint(x), __float_as_uint(x), false, false);
  return fmaxf(__uint_as_float(s[0]), __uint_as_float(s[1]));
}
__device__ __forceinline__ float x32_max(float x) {
  auto s = __builtin_amdgcn_permlane32_swap(__float_as_uint(x), __float_as_uint(x), false, false);
  return fmaxf(__uint_as_float(s[0]), __uint_as_float(s[1]));
}
__device__ __forceinline__ float wave_sum(float x) {
  x += dpp<XOR1>(x); x += dpp<XOR2>(x); x += dpp<XOR7>(x); x += dpp<XOR8>(x);
  return x32_sum(x16_sum(x));
}
__device__ __forceinline__ float silu(float x) { return x * __builtin_amdgcn_rcpf(1.f + __builtin_amdgcn_exp2f(x * -1.4426950408889634f)); }

__device__ __forceinline__ void lds_set(int addr, u32 val) {
  asm volatile("ds_write_b32 %0, %1" ::"v"(addr), "v"(val) : "memory");
}
__device__ __forceinline__ u32 lds_get(int addr) {
  u32 r;
  asm volatile("ds_read_b32 %0, %1\n\ts_waitcnt lgkmcnt(0)" : "=v"(r) : "v"(addr) : "memory");
  return __builtin_amdgcn_readfirstlane(r);
}
__device__ __forceinline__ u32 lds_wait_ge(int addr, u32 want) {
  u32 v = lds_get(addr);
  for (u32 n = 0; v < want && n < kSpinLds; ++n) { __builtin_amdgcn_s_sleep(1); v = lds_get(addr); }
  return v;
}
__device__ __forceinline__ u32 lds_spin_ge(int addr, u32 want) {
  u32 v = lds_get(addr);
  for (u32 n = 0; v < want && n < kSpinLds; ++n) v = lds_get(addr);
  return v;
}
__device__ __forceinline__ void lds_arrive(int addr, int) {
  asm volatile("s_mov_b64 exec, 1\n\tds_add_u32 %0, %1\n\ts_mov_b64 exec, -1" ::"v"(addr), "v"(1u) : "memory");
}
__device__ __forceinline__ v4u ws_ld(rsrc_t rs, int off) { return __builtin_amdgcn_raw_buffer_load_b128(rs, off, 0, AUX_SC1); }
__device__ __forceinline__ void ws_st(rsrc_t rs, int off, v4u v) { __builtin_amdgcn_raw_buffer_store_b128(v, rs, off, 0, AUX_SC1); }
__device__ __forceinline__ bool tag_ok(v4u u, u32 tag) { return u.y == tag && u.w == tag; }
__device__ __forceinline__ bool all_of(bool ok) { return __builtin_amdgcn_ballot_w64(!ok) == 0ull; }
typedef int s4i __attribute__((ext_vector_type(4)));
#define POLL_REGS "v96", "v97", "v98", "v99", "v100", "v101", "v102", "v103", "v104", "v105", "v106", "v107"
__device__ __forceinline__ void poll_a(s4i rs, int off) {
  asm volatile("buffer_load_dwordx4 v[96:99], %0, %1, 0 offen sc1" ::"v"(off), "s"(rs) : "memory", POLL_REGS);
}
__device__ __forceinline__ void poll_b(s4i rs, int off) {
  asm volatile("buffer_load_dwordx4 v[100:103], %0, %1, 0 offen sc1" ::"v"(off), "s"(rs) : "memory", POLL_REGS);
}
__device__ __forceinline__ void poll_c(s4i rs, int off) {
  asm volatile("buffer_load_dwordx4 v[104:107], %0, %1, 0 offen sc1" ::"v"(off), "s"(rs) : "memory", POLL_REGS);
}
__device__ __forceinline__ v4u take_c() {
  v4u u;
  asm volatile("s_waitcnt vmcnt(2)\n\tv_mov_b32 %0, v104\n\tv_mov_b32 %1, v105\n\tv_mov_b32 %2, v106\n\tv_mov_b32 %3, v107"
               : "=v"(u.x), "=v"(u.y), "=v"(u.z), "=v"(u.w)::"memory", POLL_REGS);
  return u;
}
__device__ __forceinline__ v2u poll_end(s4i rs, int off, u32 tag, u32& left) {
  u32 x, z, cnt;
  int ad;
  u64 ok, t;
#define POLL_STEP(r0, r1, r2, r3)                                                                   \
  "s_waitcnt vmcnt(1)\n\t"                                                                          \
  "v_cmp_eq_u32_e32 vcc, %[tag], v" #r1 "\n\t"                                                      \
  "v_cmp_eq_u32_e64 %[t], %[tag], v" #r3 "\n\t"                                                     \
  "s_and_b64 vcc, vcc, %[t]\n\t"                                                                    \
  "s_andn2_b64 vcc, vcc, %[ok]\n\t"                                                                 \
  "v_cndmask_b32_e32 %[x], %[x], v" #r0 ", vcc\n\t"                                                 \
  "v_cndmask_b32_e32 %[z], %[z], v" #r2 ", vcc\n\t"                                                 \
  "s_or_b64 %[ok], %[ok], vcc\n\t"                                                                  \
  "s_cmp_eq_u64 %[ok], -1\n\t"                                                                      \
  "s_cbranch_scc1 .Lpoll_done_%=\n\t"                                                               \
  "v_cndmask_b32_e64 %[ad], %[off], %[oob], %[ok]\n\t"                                              \
  "buffer_load_dwordx4 v[" #r0 ":" #r3 "], %[ad], %[rs], 0 offen sc1\n\t"
  asm volatile("s_mov_b64 %[ok], 0\n\t"
               "s_movk_i32 %[cnt], 0x1000\n"
               ".Lpoll_loop_%=:\n\t" POLL_STEP(96, 97, 98, 99) POLL_STEP(100, 101, 102, 103)
               "s_sub_u32 %[cnt], %[cnt], 1\n\t"
               "s_cmp_lg_u32 %[cnt], 0\n\t"
               "s_cbranch_scc1 .Lpoll_loop_%=\n"
               ".Lpoll_done_%=:\n\t"
               "s_nop 0"
               : [x] "=&v"(x), [z] "=&v"(z), [ad] "=&v"(ad), [ok] "=&s"(ok), [t] "=&s"(t), [cnt] "=&s"(cnt)
               : [off] "v"(off), [oob] "v"(OOB), [rs] "s"(rs), [tag] "s"(tag)
               : "memory", "vcc", "scc", POLL_REGS);
  left = cnt;
  return v2u{x, z};
}
__device__ __forceinline__ v2u ws_take(s4i rs, int off, v4u u, u32 tag, u32& n) {
  n = 0x1001;
  if (all_of(tag_ok(u, tag))) return v2u{u.x, u.z};
  poll_a(rs, off);
  __builtin_amdgcn_s_sleep(4);
  poll_b(rs, off);
  return poll_end(rs, off, tag, n);
}
__device__ __forceinline__ u32 now() { return (u32)__builtin_amdgcn_s_memrealtime(); }
__device__ __forceinline__ void wait_until(u32 t0, u32 dt) {
  while (now() - t0 < dt) __builtin_amdgcn_s_sleep(1);
}
__device__ __forceinline__ v4u tr_frag(LAS unsigned char* p, int half) {
  const v2u a = __builtin_bit_cast(v2u, __builtin_amdgcn_ds_read_tr16_b64_v4i16((LAS s16x4*)p));
  const v2u b = __builtin_bit_cast(v2u, __builtin_amdgcn_ds_read_tr16_b64_v4i16((LAS s16x4*)(p + half)));
  return v4u{a.x, a.y, b.x, b.y};
}
}

#define LAG 5
#define LAGK 5
#define SPDELAY 8
#define TQ1 350
#define TQ2 430
#define ODELAY 8
#define PGAP 3
#define QPOLL 6
#define HPOLL 9
#define STAMP(i)
#define STAMPV(i, v)
#define STAMP_DUMP()

__global__ void __launch_bounds__(640, 3)
fused_decode_block(const u16* __restrict__ x, const u16* __restrict__ kc, const u16* __restrict__ vc, const u16* __restrict__ wq,
                   const u16* __restrict__ wo, const u16* __restrict__ wg, const u16* __restrict__ wu, const u16* __restrict__ wd,
                   u16* __restrict__ out, unsigned char* __restrict__ ws) {
  __shared__ __attribute__((aligned(16))) unsigned char lds[LDS_BYTES];
  LAS unsigned char* L = (LAS unsigned char*)lds;
  const int tid = threadIdx.x, lane = tid & 63, wave = __builtin_amdgcn_readfirstlane(tid >> 6);
  asm volatile("" ::"s"(x), "s"(kc), "s"(vc), "s"(wq), "s"(wo), "s"(wg), "s"(wu), "s"(wd), "s"(out), "s"(ws));
  const u32 t0 = now();
  const int c = blockIdx.x, g8 = c & 7, r = c >> 3;
  const int fb = c >> 5, fc = (c & 31) * 16;
  const int j = c >> 6, t64 = c & 63;
  const bool has_gu = t64 < 56;
  const int fg = (r >> 2) * 8 + g8, cg = r & 3;
  const bool has_dn = fg < 56;
  const int dt = r >> 2;
  const int ab = r >> 2, as = r & 3;
  const rsrc_t rw = __builtin_amdgcn_make_buffer_rsrc((void*)ws, 0, WS_END, RSRC3);
  const s4i rp = {(int)(u32)(u64)ws, (int)((u32)((u64)ws >> 32) & 0xffffu), WS_END, RSRC3};
  const u32 TH_K = has_gu ? 5 : 3, TH_V = TH_K + 8, TH_DN = TH_V + 9, TH_WO = TH_V + 8 + (has_dn ? 1 : 0) + 2;
  STAMP(0);

  if (__builtin_expect(wave == 0, 1)) {
    const int r8 = lane >> 3, r4 = lane >> 4;
    const int sw128 = ((lane & 7) ^ ((r4 & 3) << 1)) * 16;
    const int swV = ((lane & 15) ^ (r4 << 1)) * 16;
    const int swK = ((lane & 15) ^ r4) * 16;
    u32 cnt, lpv = 1;
    __builtin_amdgcn_s_setprio(3);
#define GLDS(rs, ldsoff, voff, soff) __builtin_amdgcn_raw_ptr_buffer_load_lds(rs, (LAS void*)(L + (ldsoff)), 16, voff, soff, 0, AUX_NT)
#define GROUP_DONE_L(LG) do { asm volatile("s_waitcnt vmcnt(%0)" ::"n"((LG) * 8) : "memory"); ++cnt; if (cnt + 1 > lpv + (LG)) { lpv = cnt + 1 - (LG); lds_set(F_LP, lpv); } } while (0)
#define GROUP_DONE() GROUP_DONE_L(LAG)
    {
      const rsrc_t rs = __builtin_amdgcn_make_buffer_rsrc((void*)x, 0, 8192, RSRC3);
#pragma unroll
      for (int i = 0; i < 8; ++i) __builtin_amdgcn_raw_ptr_buffer_load_lds(rs, (LAS void*)(L + RX + i * 1024), 16, lane * 16, i * 1024, 0, 0);
      __builtin_amdgcn_raw_ptr_buffer_load_lds(rw, (LAS void*)(L + EL), 4, lane * 4, WS_E, 0, AUX_SC1);
    }
    {
      const rsrc_t rs = __builtin_amdgcn_make_buffer_rsrc((void*)wq, 0, 512 * 4096 * 2, RSRC3);
      const int vo = r8 * 8192 + sw128, so = j * 128 * 8192 + t64 * 128;
#pragma unroll
      for (int i = 0; i < 16; ++i) GLDS(rs, RWQ + i * 1024, vo, so + i * 8 * 8192);
    }
    STAMP(1);
    if (has_gu) {
      const rsrc_t rs = __builtin_amdgcn_make_buffer_rsrc((void*)(t64 < 28 ? wg : wu), 0, 512 * kF * 2, RSRC3);
      const int vo = r8 * (kF * 2) + sw128, so = j * 128 * (kF * 2) + (t64 < 28 ? t64 : t64 - 28) * 128;
#pragma unroll
      for (int i = 0; i < 16; ++i) GLDS(rs, RA + i * 1024, vo, so + i * 8 * (kF * 2));
      __builtin_amdgcn_s_barrier();
      asm volatile("s_waitcnt vmcnt(32)" ::: "memory");
      cnt = 4;
    } else {
      __builtin_amdgcn_s_barrier();
      asm volatile("s_waitcnt vmcnt(16)" ::: "memory");
      cnt = 2;
    }
    lds_set(F_LP, 1u);
    u32 xfin;
    asm volatile("ds_read_b32 %0, %1" : "=v"(xfin) : "v"(RX + (fb * kD + fc + (lane & 7) * 2) * 2) : "memory");
    const u32 tag = lds_get(EL) + 1u;
    asm volatile("" : "+v"(xfin));
    STAMP(2);
    const size_t kvoff = ((size_t)(ab * 8 + g8) * kSK + kW0 + as * 256) * 128;
    {
      const rsrc_t rs = __builtin_amdgcn_make_buffer_rsrc((void*)(kc + kvoff), 0, 65536, RSRC3);
      const int vo = r4 * 256;
#pragma unroll 1
      for (int n = 0; n < 8; ++n) {
#pragma unroll
        for (int i = 0; i < 8; ++i) GLDS(rs, RK + n * 8192 + i * 1024, vo + (swK ^ ((i & 3) * 64)), n * 8192 + i * 1024);
        GROUP_DONE_L(LAGK);
      }
    }
    STAMP(3);
    {
      const rsrc_t rs = __builtin_amdgcn_make_buffer_rsrc((void*)(vc + kvoff), 0, 65536, RSRC3);
      const int vo = r4 * 256;
#pragma unroll 1
      for (int n = 0; n < 8; ++n) {
        if (n == 6) lds_wait_ge(F_WQ, 4);
#pragma unroll
        for (int i = 0; i < 8; ++i) GLDS(rs, RV + n * 8192 + i * 1024, vo + (swV ^ ((i & 1) * 128)), n * 8192 + i * 1024);
        GROUP_DONE();
      }
    }
    STAMP(4);
    if (has_dn) {
      const rsrc_t rs = __builtin_amdgcn_make_buffer_rsrc((void*)wd, 0, kF * 512 * 2, RSRC3);
      const int vo = r4 * 1024, so = fg * 32 * 1024 + cg * 256;
#pragma unroll
      for (int i = 0; i < 8; ++i) GLDS(rs, RD + i * 1024, vo + (swV ^ ((i & 1) * 128)), so + i * 4096);
      GROUP_DONE();
    }
    if (has_gu) lds_wait_ge(F_GU, 4);
    {
      const rsrc_t rs = __builtin_amdgcn_make_buffer_rsrc((void*)wo, 0, 4096 * 512 * 2, RSRC3);
      const int vo = r4 * 1024, so = (g8 * 512 + dt * 16) * 1024 + cg * 256;
#pragma unroll
      for (int n = 0; n < 2; ++n) {
#pragma unroll
        for (int i = 0; i < 8; ++i)
          GLDS(rs, RA + n * 8192 + i * 1024, vo + (swV ^ ((i & 1) * 128)), so + ((2 * n + (i >> 2)) * 128 + (i & 3) * 4) * 1024);
        GROUP_DONE();
      }
    }
    STAMP(5);
    asm volatile("s_waitcnt vmcnt(32)" ::: "memory"); lds_set(F_LP, cnt - 3);
    asm volatile("s_waitcnt vmcnt(24)" ::: "memory"); lds_set(F_LP, cnt - 2);
    STAMP(6);
    asm volatile("s_waitcnt vmcnt(16)" ::: "memory"); lds_set(F_LP, cnt - 1);
    asm volatile("s_waitcnt vmcnt(8)" ::: "memory");  lds_set(F_LP, cnt);
    asm volatile("s_waitcnt vmcnt(0)" ::: "memory");  lds_set(F_LP, cnt + 1);
    STAMP(7);
    __builtin_amdgcn_s_barrier();
    float f0, f1;
    {
      const int doff = WS_DP + (((lane >> 3) * 8 + fb) * 512 + fc + (lane & 7) * 2) * 8;
      for (u32 n = 0; n < kSpinGlb; ++n) {
        asm volatile("" ::: "memory");
        v4u u[7];
#pragma unroll
        for (int i = 0; i < 7; ++i) u[i] = ws_ld(rw, doff + i * (8 * 8 * 512 * 8));
        bool ok = true;
        f0 = 0.f; f1 = 0.f;
#pragma unroll
        for (int i = 0; i < 7; ++i) { ok = ok && tag_ok(u[i], tag); f0 += __uint_as_float(u[i].x); f1 += __uint_as_float(u[i].z); }
        STAMPV(20, n);
        if (all_of(ok)) break;
      }
      f0 += dpp<XOR8>(f0); f1 += dpp<XOR8>(f1);
      f0 = rbf(silu(rbf(x32_sum(x16_sum(f0)))));
      f1 = rbf(silu(rbf(x32_sum(x16_sum(f1)))));
      asm volatile("" ::"v"(f0), "v"(f1));
    }
    STAMP(8);
    __builtin_amdgcn_s_barrier();
    asm volatile("" ::: "memory");
    LAS f32x4* fr = (LAS f32x4*)(L + FR + (lane & 7) * 64);
    const f32x4 a0 = fr[0], a1 = fr[1], a2 = fr[2], a3 = fr[3];
    const float s0 = rbf(((a0[0] + a0[1]) + (a0[2] + a0[3])) + ((a1[0] + a1[1]) + (a1[2] + a1[3])));
    const float s1 = rbf(((a2[0] + a2[1]) + (a2[2] + a2[3])) + ((a3[0] + a3[1]) + (a3[2] + a3[3])));
    if (lane < 8) ((GAS u32*)out)[(fb * kD + fc) / 2 + lane] = pk(bflo(xfin) + rbf(s0 * f0), bfhi(xfin) + rbf(s1 * f1));
    if (c == 0 && lane == 0) __builtin_amdgcn_raw_buffer_store_b32(tag, rw, WS_E, 0, AUX_SC1);
    STAMP(9);
    STAMP_DUMP();
    return;
  }

  const int cw = wave - 1, lc = lane & 15, lg = lane >> 4;
  if (cw == 0 && lane < 8) lds_set(FL + lane * 4, 0u);
  asm volatile("s_waitcnt lgkmcnt(0)" ::: "memory");
  __builtin_amdgcn_s_barrier();
  const int tq = lc >> 2, tp = lc & 3, row0 = 4 * lg + tq;
  if (wave == 9) {
    if (!has_dn) return;
    wait_until(t0, TQ2);
    lds_wait_ge(F_LP, 1);
    const u32 tag = lds_get(EL) + 1u;
    const int hoff = WS_HP + ((lane >> 3) * 896 + fg * 16 + (lane & 7) * 2) * 8;
    float g[4], u[4];
    for (u32 n = 0; n < kSpinGlb; ++n) {
      asm volatile("" ::: "memory");
      v4u t[8];
#pragma unroll
      for (int i = 0; i < 8; ++i) t[i] = ws_ld(rw, hoff + i * (8 * 896 * 8));
      bool ok = true;
#pragma unroll
      for (int i = 0; i < 8; ++i) ok = ok && tag_ok(t[i], tag);
      g[0] = (bflo(t[0].x) + bflo(t[2].x)) + (bflo(t[4].x) + bflo(t[6].x)); u[0] = (bflo(t[1].x) + bflo(t[3].x)) + (bflo(t[5].x) + bflo(t[7].x));
      g[1] = (bfhi(t[0].x) + bfhi(t[2].x)) + (bfhi(t[4].x) + bfhi(t[6].x)); u[1] = (bfhi(t[1].x) + bfhi(t[3].x)) + (bfhi(t[5].x) + bfhi(t[7].x));
      g[2] = (bflo(t[0].z) + bflo(t[2].z)) + (bflo(t[4].z) + bflo(t[6].z)); u[2] = (bflo(t[1].z) + bflo(t[3].z)) + (bflo(t[5].z) + bflo(t[7].z));
      g[3] = (bfhi(t[0].z) + bfhi(t[2].z)) + (bfhi(t[4].z) + bfhi(t[6].z)); u[3] = (bfhi(t[1].z) + bfhi(t[3].z)) + (bfhi(t[5].z) + bfhi(t[7].z));
      STAMPV(20, n);
      if (all_of(ok)) break;
    }
#pragma unroll
    for (int i = 0; i < 4; ++i) g[i] = rbf(silu(rbf(g[i]))) * rbf(u[i]);
    *(LAS v2u*)(L + HL + ((lane & 3) * 8 + (lane >> 3)) * 16 + ((lane >> 2) & 1) * 8) = v2u{pk(g[0], g[1]), pk(g[2], g[3])};
    const v4u hf = *(LAS v4u*)(L + HL + (lg * 8 + (lc & 7)) * 16);
    const int off = WS_DP + ((fg * 8 + (lc & 7)) * 512 + cg * 128 + lg * 4 + (lc >> 3) * 2) * 8;
    STAMP(1);
    lds_wait_ge(F_LP, TH_DN);
    STAMP(2);
#pragma unroll
    for (int i = 0; i < 8; ++i) {
      const f32x4 acc = mfma(tr_frag(L + RD + row0 * 256 + (((2 * i + (tp >> 1)) ^ ((row0 & 7) << 1)) * 16) + (tp & 1) * 8, 4096), hf,
                             f32x4{0.f, 0.f, 0.f, 0.f});
      ws_st(rw, off + i * 128, v4u{__float_as_uint(lc < 8 ? acc[0] : acc[2]), tag, __float_as_uint(lc < 8 ? acc[1] : acc[3]), tag});
    }
    STAMP(3);
    STAMP_DUMP();
    return;
  }
  u32 lp = lds_spin_ge(F_LP, 1);
  STAMP(1);
  const u32 tag = lds_get(EL) + 1u;
  {
    const v4u xr = *(LAS v4u*)(L + RX + cw * 1024 + lane * 16);
    float v[8];
#pragma unroll
    for (int i = 0; i < 4; ++i) { v[2 * i] = bflo(xr[i]); v[2 * i + 1] = bfhi(xr[i]); }
    const float x0 = __builtin_bit_cast(float, __builtin_amdgcn_readfirstlane(xr[0] << 16));
    float s0 = 0.f, q0 = 0.f;
#pragma unroll
    for (int i = 0; i < 8; ++i) { const float d = v[i] - x0; s0 += d; q0 = fmaf(d, d, q0); }
    s0 = wave_sum(s0) * (1.f / kD);
    q0 = wave_sum(q0) * (1.f / kD);
    const float mb = rbf(x0 + s0), vb = rbf(fmaxf(fmaf(-s0, s0, q0), 0.f));
    const float rden = __builtin_amdgcn_rcpf(rbf(__builtin_amdgcn_sqrtf(rbf(vb + 1e-5f))));
#pragma unroll
    for (int i = 0; i < 8; ++i) v[i] = rbf(v[i] - mb) * rden;
    if (lg == j) {
      const int ks = lc >> 2, o = lc & 3, gq = (o & 1) * 2, hf = o >> 1;
      LAS v2u* p = (LAS v2u*)(L + XN + ((ks * 4 + gq) * 8 + cw) * 16 + hf * 8);
      p[0] = v2u{pk(v[0], v[1]), pk(v[2], v[3])};
      p[16] = v2u{pk(v[4], v[5]), pk(v[6], v[7])};
    }
    lds_arrive(F_XN, lane);
  }
  STAMP(2);
  if (cw < 4 || has_gu) {
    const int nt = cw & 3;
    lds_spin_ge(F_XN, 8);
    v4u xf[4];
#pragma unroll
    for (int ks = 0; ks < 4; ++ks) xf[ks] = *(LAS v4u*)(L + XN + ((ks * 4 + lg) * 8 + (lc & 7)) * 16);
    lp = lds_spin_ge(F_LP, cw < 4 ? 3u : 5u);
    STAMP(3);
    LAS unsigned char* base = L + (cw < 4 ? RWQ : RA) + row0 * 128 + (((2 * nt + (tp >> 1)) ^ (((row0 >> 1) & 3) << 1)) * 16) + (tp & 1) * 8;
    f32x4 acc = {0.f, 0.f, 0.f, 0.f};
#pragma unroll
    for (int ks = 0; ks < 4; ++ks) acc = mfma(tr_frag(base + ks * 4096, 2048), xf[ks], acc);
    if (lc < 8) {
      const v4u o = {pk(acc[0], acc[1]), tag, pk(acc[2], acc[3]), tag};
      const int off = cw < 4 ? WS_QP + ((j * 8 + lc) * 2048 + t64 * 32 + nt * 8 + lg * 2) * 8
                             : WS_HP + (((j * 2 + (t64 >= 28)) * 8 + lc) * 896 + (t64 >= 28 ? t64 - 28 : t64) * 32 + nt * 8 + lg * 2) * 8;
      ws_st(rw, off, o);
    }
    lds_arrive(cw < 4 ? F_WQ : F_GU, lane);
    STAMP(4);
  }
  u32 n;
  {
    const int qoff = WS_QP + ((lg * 8 + ab) * 2048 + g8 * 256 + (cw * 16 + lc) * 2) * 8;
    wait_until(t0, TQ1);
    STAMP(5);
    const v4u uq4 = ws_ld(rw, qoff);
    const v2u uq = ws_take(rp, qoff, uq4, tag, n);
    STAMPV(20, 0x1001 - n);
    STAMP(6);
    const float q0 = x32_sum(x16_sum(bflo(uq.x))), q1 = x32_sum(x16_sum(bfhi(uq.x))), q2 = x32_sum(x16_sum(bflo(uq.y))),
                q3 = x32_sum(x16_sum(bfhi(uq.y)));
    if (lg == 0) {
      const int d = cw * 64 + lc * 4;
      *(LAS v2u*)(L + QL + (d >> 7) * 272 + (d & 127) * 2) = v2u{pk(q0, q1), pk(q2, q3)};
    }
    lds_arrive(F_Q, lane);
  }
  {
    if (lp < TH_K + cw + 1) lp = lds_wait_ge(F_LP, TH_K + cw + 1);
    v4u kf[8];
#pragma unroll
    for (int i = 0; i < 8; ++i) kf[i] = *(LAS v4u*)(L + RK + (cw * 32 + (i >> 2) * 16 + lc) * 256 + ((((i & 3) * 4 + lg) ^ lc) * 16));
    lds_spin_ge(F_Q, 8);
    STAMP(7);
    v4u qf[4];
#pragma unroll
    for (int ks = 0; ks < 4; ++ks) qf[ks] = *(LAS v4u*)(L + QL + (lc & 3) * 272 + ks * 64 + lg * 16);
    f32x4 a0 = {0.f, 0.f, 0.f, 0.f}, a1 = {0.f, 0.f, 0.f, 0.f};
#pragma unroll
    for (int ks = 0; ks < 4; ++ks) { a0 = mfma(kf[ks], qf[ks], a0); a1 = mfma(kf[4 + ks], qf[ks], a1); }
    float sc[8];
#pragma unroll
    for (int i = 0; i < 4; ++i) { sc[i] = rbf(rbf(a0[i]) * 0.08838834764831845f); sc[4 + i] = rbf(rbf(a1[i]) * 0.08838834764831845f); }
    float m = fmaxf(fmaxf(fmaxf(sc[0], sc[1]), fmaxf(sc[2], sc[3])), fmaxf(fmaxf(sc[4], sc[5]), fmaxf(sc[6], sc[7])));
    m = x32_max(x16_max(m));
#pragma unroll
    for (int i = 0; i < 8; ++i) sc[i] = __builtin_amdgcn_exp2f((sc[i] - m) * 1.4426950408889634f);
    if (lc < 4) *(LAS v4u*)(L + PL + ((cw * 4 + lg) * 4 + lc) * 16) = v4u{pk(sc[0], sc[1]), pk(sc[2], sc[3]), pk(sc[4], sc[5]), pk(sc[6], sc[7])};
    const float l = x32_sum(x16_sum(((sc[0] + sc[1]) + (sc[2] + sc[3])) + ((sc[4] + sc[5]) + (sc[6] + sc[7]))));
    if (lane < 4) { *(LAS float*)(L + MLM + (lane * 8 + cw) * 4) = m; *(LAS float*)(L + MLL + (lane * 8 + cw) * 4) = l; }
    lds_arrive(F_P, lane);
  }
  lds_spin_ge(F_P, 8);
  STAMP(8);
  const int wb = row0 * 256 + (((2 * cw + (tp >> 1)) ^ ((row0 & 7) << 1)) * 16) + (tp & 1) * 8;
  int opw = WS_OP + ((((g8 * 8 + cw) * 8 + ab) * 4 + as) * 16 + lc * 4 + lg) * 16;
  int ooff = WS_OP + ((((g8 * 8 + dt) * 8 + cw) * 4 + lg) * 16 + lc) * 16;
  int moff = WS_ML + (((cw * 8 + g8) * 4 + lg) * 4 + (lc >> 2)) * 16;
  int spw = WS_SP + (((g8 * 8 + dt) * 8 + (lc & 7)) * 512 + cg * 128 + cw * 16 + lg * 4 + (lc >> 3) * 2) * 8;
  int soff = WS_SP + (((cw * 8 + (lane >> 3)) * 8 + fb) * 512 + fc + (lane & 7) * 2) * 8;
  int olw = OL + (((lc >> 3) * 4 + (lc & 3)) * 8 + cw) * 16 + ((lc >> 2) & 1) * 8, olr = OL + (lg * 8 + (lc & 7)) * 16;
  int frw = FR + (lane & 7) * 64 + cw * 4;
  {
    const f32x4 m0 = *(LAS f32x4*)(L + MLM + (lc & 3) * 32), m1 = *(LAS f32x4*)(L + MLM + (lc & 3) * 32 + 16);
    v4u pf[8];
#pragma unroll
    for (int i = 0; i < 8; ++i) pf[i] = *(LAS v4u*)(L + PL + ((i * 4 + lg) * 4 + (lc & 3)) * 16);
    const float M = fmaxf(fmaxf(fmaxf(m0[0], m0[1]), fmaxf(m0[2], m0[3])), fmaxf(fmaxf(m1[0], m1[1]), fmaxf(m1[2], m1[3])));
    float f[8];
#pragma unroll
    for (int i = 0; i < 4; ++i) {
      f[i] = __builtin_amdgcn_exp2f((m0[i] - M) * 1.4426950408889634f);
      f[4 + i] = __builtin_amdgcn_exp2f((m1[i] - M) * 1.4426950408889634f);
    }
    if (cw == 0 && lane < 4) {
      const f32x4 l0 = *(LAS f32x4*)(L + MLL + lane * 32), l1 = *(LAS f32x4*)(L + MLL + lane * 32 + 16);
      const float l = (fmaf(l0[0], f[0], l0[1] * f[1]) + fmaf(l0[2], f[2], l0[3] * f[3])) + (fmaf(l1[0], f[4], l1[1] * f[5]) + fmaf(l1[2], f[6], l1[3] * f[7]));
      ws_st(rw, WS_ML + (((ab * 8 + g8) * 4 + as) * 4 + lane) * 16, v4u{__float_as_uint(M), tag, __float_as_uint(l), tag});
    }
    const f32x4 z = {0.f, 0.f, 0.f, 0.f};
    f32x4 acc = z;
    asm volatile("" : "+v"(ooff), "+v"(moff), "+v"(soff), "+v"(spw), "+v"(opw), "+v"(olw), "+v"(olr), "+v"(frw));
    if (lp < TH_V + 5) lp = lds_wait_ge(F_LP, TH_V + 5);
#pragma unroll
    for (int i = 0; i < 5; ++i) acc += f[i] * mfma(tr_frag(L + RV + wb + i * 8192, 4096), pf[i], z);
#pragma unroll
    for (int i = 5; i < 8; ++i) {
      if (lp < TH_V + i + 1) lp = lds_spin_ge(F_LP, TH_V + i + 1);
      acc += f[i] * mfma(tr_frag(L + RV + wb + i * 8192, 4096), pf[i], z);
    }
    if (lc < 4)
      ws_st(rw, opw, v4u{pk(acc[0], acc[1]), tag, pk(acc[2], acc[3]), tag});
    poll_c(rp, moff);
    STAMP(9);
  }
  {
    __builtin_amdgcn_s_sleep(ODELAY);
    STAMP(10);
    poll_a(rp, ooff);
    __builtin_amdgcn_s_sleep(PGAP);
    poll_b(rp, ooff);
    v4u um = take_c();
    if (__builtin_expect(!all_of(tag_ok(um, tag)), 0)) {
      n = 0;
      do {
        asm volatile("" ::: "memory");
        um = ws_ld(rw, moff);
      } while (!all_of(tag_ok(um, tag)) && ++n < kSpinGlb);
      STAMPV(25, n + 1);
    }
    const float ms = __uint_as_float(um.x), Mx = x32_max(x16_max(ms));
    const float w = __builtin_amdgcn_exp2f((ms - Mx) * 1.4426950408889634f);
    const float sc2 = w * __builtin_amdgcn_rcpf(x32_sum(x16_sum(w * __uint_as_float(um.z))));
    lds_spin_ge(F_LP, TH_WO);
    const v4u wf0 = tr_frag(L + RA + wb, 4096), wf1 = tr_frag(L + RA + wb + 8192, 4096);
    asm volatile("" ::"v"(sc2), "v"(wf0), "v"(wf1));
    const v2u uo = poll_end(rp, ooff, tag, n);
    STAMPV(22, 0x1000 - n);
    STAMP(11);
    const float o0 = x32_sum(x16_sum(sc2 * bflo(uo.x))), o1 = x32_sum(x16_sum(sc2 * bfhi(uo.x))),
                o2 = x32_sum(x16_sum(sc2 * bflo(uo.y))), o3 = x32_sum(x16_sum(sc2 * bfhi(uo.y)));
    if (lg == 0)
      *(LAS v2u*)(L + olw) = v2u{pk(o0, o1), pk(o2, o3)};
    asm volatile("s_waitcnt lgkmcnt(0)" ::: "memory");
    __builtin_amdgcn_s_barrier();
    asm volatile("" ::: "memory");
    f32x4 acc = mfma(wf0, *(LAS v4u*)(L + olr), f32x4{0.f, 0.f, 0.f, 0.f});
    acc = mfma(wf1, *(LAS v4u*)(L + olr + 512), acc);
    ws_st(rw, spw, v4u{__float_as_uint(lc < 8 ? acc[0] : acc[2]), tag, __float_as_uint(lc < 8 ? acc[1] : acc[3]), tag});
  }
  STAMP(12);
  {
    __builtin_amdgcn_s_sleep(SPDELAY);
    STAMP(13);
    poll_a(rp, soff);
    __builtin_amdgcn_s_sleep(PGAP);
    poll_b(rp, soff);
    const v2u us = poll_end(rp, soff, tag, n);
    STAMPV(23, 0x1000 - n);
    STAMP(14);
    float s0 = __uint_as_float(us.x), s1 = __uint_as_float(us.y);
    s0 += dpp<XOR8>(s0); s1 += dpp<XOR8>(s1);
    s0 = x32_sum(x16_sum(s0)); s1 = x32_sum(x16_sum(s1));
    if (lane < 8) { *(LAS float*)(L + frw) = s0; *(LAS float*)(L + frw + 32) = s1; }
    asm volatile("s_waitcnt lgkmcnt(0)" ::: "memory");
    __builtin_amdgcn_s_barrier();
    STAMP(15);
    STAMP_DUMP();
  }
}

__global__ void p_small(unsigned char* ws) { if (ws == nullptr) ws[0] = 1; }
__global__ void __launch_bounds__(640, 3) p_biglds(unsigned char* ws) {
  __shared__ unsigned char l[163840];
  l[threadIdx.x] = (unsigned char)threadIdx.x;
  __syncthreads();
  if (ws == nullptr) ws[0] = l[threadIdx.x ^ 1];
}
__global__ void __launch_bounds__(640, 3) p_big(unsigned char* ws) { if (ws == nullptr) ws[0] = 1; }
extern "C" void kernel_launch(void* const* d_in, const int* in_sizes, int n_in, void* d_out, int out_size, void* d_ws,
                              size_t ws_size, hipStream_t stream) {
  fused_decode_block<<<256, 640, 0, stream>>>((const u16*)d_in[0], (const u16*)d_in[1], (const u16*)d_in[2], (const u16*)d_in[3],
                                              (const u16*)d_in[4], (const u16*)d_in[6], (const u16*)d_in[7], (const u16*)d_in[8],
                                              (u16*)d_out, (unsigned char*)d_ws);
  p_big<<<256, 640, 0, stream>>>((unsigned char*)d_ws);
}
```

```cpp
#include <hip/hip_runtime.h>

typedef unsigned short u16;
typedef unsigned u32;
typedef unsigned long long u64;
typedef u32 v4u __attribute__((ext_vector_type(4)));
typedef u32 v2u __attribute__((ext_vector_type(2)));
typedef float f32x4 __attribute__((ext_vector_type(4)));
typedef short s16x4 __attribute__((ext_vector_type(4)));
typedef __bf16 bf16x8 __attribute__((ext_vector_type(8)));
typedef __bf16 bf16x2 __attribute__((ext_vector_type(2)));
#define LAS __attribute__((address_space(3)))
#define GAS __attribute__((address_space(1)))
typedef __amdgpu_buffer_rsrc_t rsrc_t;

namespace {
constexpr int kD = 512, kSK = 4096, kW0 = 3072, kF = 1792;
constexpr int RK = 0, RV = 65536, RWQ = RV + 49152, RA = 131072, RD = 147456, RM = 155648;
constexpr int XN = RM, OL = RM, FR = RM + 1024, QL = RM + 2048, PL = RM + 3328, MLM = RM + 5376, MLL = RM + 5504,
              HL = RM + 5632, FL = RM + 6144;
constexpr int F_LP = FL, F_XN = FL + 4, F_WQ = FL + 8, F_GU = FL + 12, F_Q = FL + 16, F_D = FL + 20, F_P = FL + 24, F_H = FL + 28;
constexpr int EL = FL + 64, ST = FL + 320, RX = RD;
constexpr int LDS_BYTES = 163840;
constexpr int WS_E = 0, WS_QP = 4096, WS_HP = WS_QP + 4 * 8 * 2048 * 8, WS_OP = WS_HP + 4 * 2 * 8 * 896 * 8,
              WS_ML = WS_OP + 8 * 32 * 4 * 64 * 8, WS_SP = WS_ML + 8 * 32 * 4 * 16, WS_DP = WS_SP + 64 * 8 * 512 * 8,
              WS_END = WS_DP + 56 * 8 * 512 * 8;
constexpr int OOB = 0x7ffffff0;
constexpr int AUX_NT = 2, AUX_SC1 = 16, RSRC3 = 0x00020000;
constexpr u32 kSpinLds = 1u << 22, kSpinGlb = 1u << 16;

__device__ __forceinline__ float bflo(u32 w) { return __builtin_bit_cast(float, w << 16); }
__device__ __forceinline__ float bfhi(u32 w) { return __builtin_bit_cast(float, w & 0xffff0000u); }
__device__ __forceinline__ u32 pk(float lo, float hi) {
  bf16x2 v = {(__bf16)lo, (__bf16)hi};
  return __builtin_bit_cast(u32, v);
}
__device__ __forceinline__ float rbf(float f) { return (float)(__bf16)f; }
__device__ __forceinline__ f32x4 mfma(v4u a, v4u b, f32x4 c) {
  return __builtin_amdgcn_mfma_f32_16x16x32_bf16(__builtin_bit_cast(bf16x8, a), __builtin_bit_cast(bf16x8, b), c, 0, 0, 0);
}
template <int CTRL> __device__ __forceinline__ float dpp(float x) {
  return __builtin_bit_cast(float, __builtin_amdgcn_mov_dpp(__builtin_bit_cast(int, x), CTRL, 0xf, 0xf, true));
}
constexpr int XOR1 = 0xB1, XOR2 = 0x4E, XOR7 = 0x141, XOR8 = 0x128, ROR4 = 0x124;
__device__ __forceinline__ float x16_sum(float x) {
  auto s = __builtin_amdgcn_permlane16_swap(__float_as_uint(x), __float_as_uint(x), false, false);
  return __uint_as_float(s[0]) + __uint_as_float(s[1]);
}
__device__ __forceinline__ float x32_sum(float x) {
  auto s = __builtin_amdgcn_permlane32_swap(__float_as_uint(x), __float_as_uint(x), false, false);
  return __uint_as_float(s[0]) + __uint_as_float(s[1]);
}
__device__ __forceinline__ float x16_max(float x) {
  auto s = __builtin_amdgcn_permlane16_swap(__float_as_uint(x), __float_as_uint(x), false, false);
  return fmaxf(__uint_as_float(s[0]), __uint_as_float(s[1]));
}
__device__ __forceinline__ float x32_max(float x) {
  auto s = __builtin_amdgcn_permlane32_swap(__float_as_uint(x), __float_as_uint(x), false, false);
  return fmaxf(__uint_as_float(s[0]), __uint_as_float(s[1]));
}
__device__ __forceinline__ float wave_sum(float x) {
  x += dpp<XOR1>(x); x += dpp<XOR2>(x); x += dpp<XOR7>(x); x += dpp<XOR8>(x);
  return x32_sum(x16_sum(x));
}
__device__ __forceinline__ float silu(float x) { return x * __builtin_amdgcn_rcpf(1.f + __builtin_amdgcn_exp2f(x * -1.4426950408889634f)); }

__device__ __forceinline__ void lds_set(int addr, u32 val) {
  asm volatile("ds_write_b32 %0, %1" ::"v"(addr), "v"(val) : "memory");
}
__device__ __forceinline__ u32 lds_get(int addr) {
  u32 r;
  asm volatile("ds_read_b32 %0, %1\n\ts_waitcnt lgkmcnt(0)" : "=v"(r) : "v"(addr) : "memory");
  return __builtin_amdgcn_readfirstlane(r);
}
__device__ __forceinline__ u32 lds_wait_ge(int addr, u32 want) {
  u32 v = lds_get(addr);
  for (u32 n = 0; v < want && n < kSpinLds; ++n) { __builtin_amdgcn_s_sleep(1); v = lds_get(addr); }
  return v;
}
__device__ __forceinline__ u32 lds_spin_ge(int addr, u32 want) {
  u32 v = lds_get(addr);
  for (u32 n = 0; v < want && n < kSpinLds; ++n) v = lds_get(addr);
  return v;
}
__device__ __forceinline__ void lds_arrive(int addr, int) {
  asm volatile("s_mov_b64 exec, 1\n\tds_add_u32 %0, %1\n\ts_mov_b64 exec, -1" ::"v"(addr), "v"(1u) : "memory");
}
__device__ __forceinline__ v4u ws_ld(rsrc_t rs, int off) { return __builtin_amdgcn_raw_buffer_load_b128(rs, off, 0, AUX_SC1); }
__device__ __forceinline__ void ws_st(rsrc_t rs, int off, v4u v) { __builtin_amdgcn_raw_buffer_store_b128(v, rs, off, 0, AUX_SC1); }
__device__ __forceinline__ bool tag_ok(v4u u, u32 tag) { return u.y == tag && u.w == tag; }
__device__ __forceinline__ bool all_of(bool ok) { return __builtin_amdgcn_ballot_w64(!ok) == 0ull; }
typedef int s4i __attribute__((ext_vector_type(4)));
#define POLL_REGS "v96", "v97", "v98", "v99", "v100", "v101", "v102", "v103", "v104", "v105", "v106", "v107"
__device__ __forceinline__ void poll_a(s4i rs, int off) {
  asm volatile("buffer_load_dwordx4 v[96:99], %0, %1, 0 offen sc1" ::"v"(off), "s"(rs) : "memory", POLL_REGS);
}
__device__ __forceinline__ void poll_b(s4i rs, int off) {
  asm volatile("buffer_load_dwordx4 v[100:103], %0, %1, 0 offen sc1" ::"v"(off), "s"(rs) : "memory", POLL_REGS);
}
__device__ __forceinline__ void poll_c(s4i rs, int off) {
  asm volatile("buffer_load_dwordx4 v[104:107], %0, %1, 0 offen sc1" ::"v"(off), "s"(rs) : "memory", POLL_REGS);
}
__device__ __forceinline__ v4u take_c() {
  v4u u;
  asm volatile("s_waitcnt vmcnt(2)\n\tv_mov_b32 %0, v104\n\tv_mov_b32 %1, v105\n\tv_mov_b32 %2, v106\n\tv_mov_b32 %3, v107"
               : "=v"(u.x), "=v"(u.y), "=v"(u.z), "=v"(u.w)::"memory", POLL_REGS);
  return u;
}
__device__ __forceinline__ v2u poll_end(s4i rs, int off, u32 tag, u32& left) {
  u32 x, z, cnt;
  int ad;
  u64 ok, t;
#define POLL_STEP(r0, r1, r2, r3)                                                                   \
  "s_waitcnt vmcnt(1)\n\t"                                                                          \
  "v_cmp_eq_u32_e32 vcc, %[tag], v" #r1 "\n\t"                                                      \
  "v_cmp_eq_u32_e64 %[t], %[tag], v" #r3 "\n\t"                                                     \
  "s_and_b64 vcc, vcc, %[t]\n\t"                                                                    \
  "s_andn2_b64 vcc, vcc, %[ok]\n\t"                                                                 \
  "v_cndmask_b32_e32 %[x], %[x], v" #r0 ", vcc\n\t"                                                 \
  "v_cndmask_b32_e32 %[z], %[z], v" #r2 ", vcc\n\t"                                                 \
  "s_or_b64 %[ok], %[ok], vcc\n\t"                                                                  \
  "s_cmp_eq_u64 %[ok], -1\n\t"                                                                      \
  "s_cbranch_scc1 .Lpoll_done_%=\n\t"                                                               \
  "v_cndmask_b32_e64 %[ad], %[off], %[oob], %[ok]\n\t"                                              \
  "buffer_load_dwordx4 v[" #r0 ":" #r3 "], %[ad], %[rs], 0 offen sc1\n\t"
  asm volatile("s_mov_b64 %[ok], 0\n\t"
               "s_movk_i32 %[cnt], 0x1000\n"
               ".Lpoll_loop_%=:\n\t" POLL_STEP(96, 97, 98, 99) POLL_STEP(100, 101, 102, 103)
               "s_sub_u32 %[cnt], %[cnt], 1\n\t"
               "s_cmp_lg_u32 %[cnt], 0\n\t"
               "s_cbranch_scc1 .Lpoll_loop_%=\n"
               ".Lpoll_done_%=:\n\t"
               "s_nop 0"
               : [x] "=&v"(x), [z] "=&v"(z), [ad] "=&v"(ad), [ok] "=&s"(ok), [t] "=&s"(t), [cnt] "=&s"(cnt)
               : [off] "v"(off), [oob] "v"(OOB), [rs] "s"(rs), [tag] "s"(tag)
               : "memory", "vcc", "scc", POLL_REGS);
  left = cnt;
  return v2u{x, z};
}
__device__ __forceinline__ v2u ws_take(s4i rs, int off, v4u u, u32 tag, u32& n) {
  n = 0x1001;
  if (all_of(tag_ok(u, tag))) return v2u{u.x, u.z};
  poll_a(rs, off);
  __builtin_amdgcn_s_sleep(4);
  poll_b(rs, off);
  return poll_end(rs, off, tag, n);
}
__device__ __forceinline__ u32 now() { return (u32)__builtin_amdgcn_s_memrealtime(); }
__device__ __forceinline__ void wait_until(u32 t0, u32 dt) {
  while (now() - t0 < dt) __builtin_amdgcn_s_sleep(1);
}
__device__ __forceinline__ v4u tr_frag(LAS unsigned char* p, int half) {
  const v2u a = __builtin_bit_cast(v2u, __builtin_amdgcn_ds_read_tr16_b64_v4i16((LAS s16x4*)p));
  const v2u b = __builtin_bit_cast(v2u, __builtin_amdgcn_ds_read_tr16_b64_v4i16((LAS s16x4*)(p + half)));
  return v4u{a.x, a.y, b.x, b.y};
}
}

#define LAG 5
#define LAGK 5
#define SPDELAY 8
#define TQ1 350
#define TQ2 430
#define ODELAY 8
#define PGAP 3
#define QPOLL 6
#define HPOLL 9
#define STAMP(i)
#define STAMPV(i, v)
#define STAMP_DUMP()

__global__ void __launch_bounds__(640, 3)
fused_decode_block(const u16* __restrict__ x, const u16* __restrict__ kc, const u16* __restrict__ vc, const u16* __restrict__ wq,
                   const u16* __restrict__ wo, const u16* __restrict__ wg, const u16* __restrict__ wu, const u16* __restrict__ wd,
                   u16* __restrict__ out, unsigned char* __restrict__ ws) {
  __shared__ __attribute__((aligned(16))) unsigned char lds[LDS_BYTES];
  LAS unsigned char* L = (LAS unsigned char*)lds;
  const int tid = threadIdx.x, lane = tid & 63, wave = __builtin_amdgcn_readfirstlane(tid >> 6);
  asm volatile("" ::"s"(x), "s"(kc), "s"(vc), "s"(wq), "s"(wo), "s"(wg), "s"(wu), "s"(wd), "s"(out), "s"(ws));
  const u32 t0 = now();
  const int c = blockIdx.x, g8 = c & 7, r = c >> 3;
  const int fb = c >> 5, fc = (c & 31) * 16;
  const int j = c >> 6, t64 = c & 63;
  const bool has_gu = t64 < 56;
  const int fg = (r >> 2) * 8 + g8, cg = r & 3;
  const bool has_dn = fg < 56;
  const int dt = r >> 2;
  const int ab = r >> 2, as = r & 3;
  const rsrc_t rw = __builtin_amdgcn_make_buffer_rsrc((void*)ws, 0, WS_END, RSRC3);
  const s4i rp = {(int)(u32)(u64)ws, (int)((u32)((u64)ws >> 32) & 0xffffu), WS_END, RSRC3};
  const u32 TH_K = has_gu ? 5 : 3, TH_V = TH_K + 8, TH_DN = TH_V + 9, TH_WO = TH_V + 8 + (has_dn ? 1 : 0) + 2;
  STAMP(0);

  if (__builtin_expect(wave == 0, 1)) {
    const int r8 = lane >> 3, r4 = lane >> 4;
    const int sw128 = ((lane & 7) ^ ((r4 & 3) << 1)) * 16;
    const int swV = ((lane & 15) ^ (r4 << 1)) * 16;
    const int swK = ((lane & 15) ^ r4) * 16;
    u32 cnt, lpv = 1;
    __builtin_amdgcn_s_setprio(3);
#define GLDS(rs, ldsoff, voff, soff) __builtin_amdgcn_raw_ptr_buffer_load_lds(rs, (LAS void*)(L + (ldsoff)), 16, voff, soff, 0, AUX_NT)
#define GROUP_DONE_L(LG) do { asm volatile("s_waitcnt vmcnt(%0)" ::"n"((LG) * 8) : "memory"); ++cnt; if (cnt + 1 > lpv + (LG)) { lpv = cnt + 1 - (LG); lds_set(F_LP, lpv); } } while (0)
#define GROUP_DONE() GROUP_DONE_L(LAG)
    {
      const rsrc_t rs = __builtin_amdgcn_make_buffer_rsrc((void*)x, 0, 8192, RSRC3);
#pragma unroll
      for (int i = 0; i < 8; ++i) __builtin_amdgcn_raw_ptr_buffer_load_lds(rs, (LAS void*)(L + RX + i * 1024), 16, lane * 16, i * 1024, 0, 0);
      __builtin_amdgcn_raw_ptr_buffer_load_lds(rw, (LAS void*)(L + EL), 4, lane * 4, WS_E, 0, AUX_SC1);
    }
    {
      const rsrc_t rs = __builtin_amdgcn_make_buffer_rsrc((void*)wq, 0, 512 * 4096 * 2, RSRC3);
      const int vo = r8 * 8192 + sw128, so = j * 128 * 8192 + t64 * 128;
#pragma unroll
      for (int i = 0; i < 16; ++i) GLDS(rs, RWQ + i * 1024, vo, so + i * 8 * 8192);
    }
    STAMP(1);
    if (has_gu) {
      const rsrc_t rs = __builtin_amdgcn_make_buffer_rsrc((void*)(t64 < 28 ? wg : wu), 0, 512 * kF * 2, RSRC3);
      const int vo = r8 * (kF * 2) + sw128, so = j * 128 * (kF * 2) + (t64 < 28 ? t64 : t64 - 28) * 128;
#pragma unroll
      for (int i = 0; i < 16; ++i) GLDS(rs, RA + i * 1024, vo, so + i * 8 * (kF * 2));
      __builtin_amdgcn_s_barrier();
      asm volatile("s_waitcnt vmcnt(32)" ::: "memory");
      cnt = 4;
    } else {
      __builtin_amdgcn_s_barrier();
      asm volatile("s_waitcnt vmcnt(16)" ::: "memory");
      cnt = 2;
    }
    lds_set(F_LP, 1u);
    u32 xfin;
    asm volatile("ds_read_b32 %0, %1" : "=v"(xfin) : "v"(RX + (fb * kD + fc + (lane & 7) * 2) * 2) : "memory");
    const u32 tag = lds_get(EL) + 1u;
    asm volatile("" : "+v"(xfin));
    STAMP(2);
    const size_t kvoff = ((size_t)(ab * 8 + g8) * kSK + kW0 + as * 256) * 128;
    {
      const rsrc_t rs = __builtin_amdgcn_make_buffer_rsrc((void*)(kc + kvoff), 0, 65536, RSRC3);
      const int vo = r4 * 256;
#pragma unroll 1
      for (int n = 0; n < 8; ++n) {
#pragma unroll
        for (int i = 0; i < 8; ++i) GLDS(rs, RK + n * 8192 + i * 1024, vo + (swK ^ ((i & 3) * 64)), n * 8192 + i * 1024);
        GROUP_DONE_L(LAGK);
      }
    }
    STAMP(3);
    {
      const rsrc_t rs = __builtin_amdgcn_make_buffer_rsrc((void*)(vc + kvoff), 0, 65536, RSRC3);
      const int vo = r4 * 256;
#pragma unroll 1
      for (int n = 0; n < 8; ++n) {
        if (n == 6) lds_wait_ge(F_WQ, 4);
#pragma unroll
        for (int i = 0; i < 8; ++i) GLDS(rs, RV + n * 8192 + i * 1024, vo + (swV ^ ((i & 1) * 128)), n * 8192 + i * 1024);
        GROUP_DONE();
      }
    }
    STAMP(4);
    if (has_dn) {
      const rsrc_t rs = __builtin_amdgcn_make_buffer_rsrc((void*)wd, 0, kF * 512 * 2, RSRC3);
      const int vo = r4 * 1024, so = fg * 32 * 1024 + cg * 256;
#pragma unroll
      for (int i = 0; i < 8; ++i) GLDS(rs, RD + i * 1024, vo + (swV ^ ((i & 1) * 128)), so + i * 4096);
      GROUP_DONE();
    }
    if (has_gu) lds_wait_ge(F_GU, 4);
    {
      const rsrc_t rs = __builtin_amdgcn_make_buffer_rsrc((void*)wo, 0, 4096 * 512 * 2, RSRC3);
      const int vo = r4 * 1024, so = (g8 * 512 + dt * 16) * 1024 + cg * 256;
#pragma unroll
      for (int n = 0; n < 2; ++n) {
#pragma unroll
        for (int i = 0; i < 8; ++i)
          GLDS(rs, RA + n * 8192 + i * 1024, vo + (swV ^ ((i & 1) * 128)), so + ((2 * n + (i >> 2)) * 128 + (i & 3) * 4) * 1024);
        GROUP_DONE();
      }
    }
    STAMP(5);
    asm volatile("s_waitcnt vmcnt(32)" ::: "memory"); lds_set(F_LP, cnt - 3);
    asm volatile("s_waitcnt vmcnt(24)" ::: "memory"); lds_set(F_LP, cnt - 2);
    STAMP(6);
    asm volatile("s_waitcnt vmcnt(16)" ::: "memory"); lds_set(F_LP, cnt - 1);
    asm volatile("s_waitcnt vmcnt(8)" ::: "memory");  lds_set(F_LP, cnt);
    asm volatile("s_waitcnt vmcnt(0)" ::: "memory");  lds_set(F_LP, cnt + 1);
    STAMP(7);
    __builtin_amdgcn_s_barrier();
    float f0, f1;
    {
      const int doff = WS_DP + (((lane >> 3) * 8 + fb) * 512 + fc + (lane & 7) * 2) * 8;
      for (u32 n = 0; n < kSpinGlb; ++n) {
        asm volatile("" ::: "memory");
        v4u u[7];
#pragma unroll
        for (int i = 0; i < 7; ++i) u[i] = ws_ld(rw, doff + i * (8 * 8 * 512 * 8));
        bool ok = true;
        f0 = 0.f; f1 = 0.f;
#pragma unroll
        for (int i = 0; i < 7; ++i) { ok = ok && tag_ok(u[i], tag); f0 += __uint_as_float(u[i].x); f1 += __uint_as_float(u[i].z); }
        STAMPV(20, n);
        if (all_of(ok)) break;
      }
      f0 += dpp<XOR8>(f0); f1 += dpp<XOR8>(f1);
      f0 = rbf(silu(rbf(x32_sum(x16_sum(f0)))));
      f1 = rbf(silu(rbf(x32_sum(x16_sum(f1)))));
      asm volatile("" ::"v"(f0), "v"(f1));
    }
    STAMP(8);
    __builtin_amdgcn_s_barrier();
    asm volatile("" ::: "memory");
    LAS f32x4* fr = (LAS f32x4*)(L + FR + (lane & 7) * 64);
    const f32x4 a0 = fr[0], a1 = fr[1], a2 = fr[2], a3 = fr[3];
    const float s0 = rbf(((a0[0] + a0[1]) + (a0[2] + a0[3])) + ((a1[0] + a1[1]) + (a1[2] + a1[3])));
    const float s1 = rbf(((a2[0] + a2[1]) + (a2[2] + a2[3])) + ((a3[0] + a3[1]) + (a3[2] + a3[3])));
    if (lane < 8) ((GAS u32*)out)[(fb * kD + fc) / 2 + lane] = pk(bflo(xfin) + rbf(s0 * f0), bfhi(xfin) + rbf(s1 * f1));
    if (lane == 0) ((GAS u32*)(ws + (100 << 20)))[c] = now();
    if (c == 0 && lane == 0) __builtin_amdgcn_raw_buffer_store_b32(tag, rw, WS_E, 0, AUX_SC1);
    STAMP(9);
    STAMP_DUMP();
    return;
  }

  const int cw = wave - 1, lc = lane & 15, lg = lane >> 4;
  if (cw == 0 && lane < 8) lds_set(FL + lane * 4, 0u);
  asm volatile("s_waitcnt lgkmcnt(0)" ::: "memory");
  __builtin_amdgcn_s_barrier();
  const int tq = lc >> 2, tp = lc & 3, row0 = 4 * lg + tq;
  if (wave == 9) {
    if (!has_dn) return;
    wait_until(t0, TQ2);
    lds_wait_ge(F_LP, 1);
    const u32 tag = lds_get(EL) + 1u;
    const int hoff = WS_HP + ((lane >> 3) * 896 + fg * 16 + (lane & 7) * 2) * 8;
    float g[4], u[4];
    for (u32 n = 0; n < kSpinGlb; ++n) {
      asm volatile("" ::: "memory");
      v4u t[8];
#pragma unroll
      for (int i = 0; i < 8; ++i) t[i] = ws_ld(rw, hoff + i * (8 * 896 * 8));
      bool ok = true;
#pragma unroll
      for (int i = 0; i < 8; ++i) ok = ok && tag_ok(t[i], tag);
      g[0] = (bflo(t[0].x) + bflo(t[2].x)) + (bflo(t[4].x) + bflo(t[6].x)); u[0] = (bflo(t[1].x) + bflo(t[3].x)) + (bflo(t[5].x) + bflo(t[7].x));
      g[1] = (bfhi(t[0].x) + bfhi(t[2].x)) + (bfhi(t[4].x) + bfhi(t[6].x)); u[1] = (bfhi(t[1].x) + bfhi(t[3].x)) + (bfhi(t[5].x) + bfhi(t[7].x));
      g[2] = (bflo(t[0].z) + bflo(t[2].z)) + (bflo(t[4].z) + bflo(t[6].z)); u[2] = (bflo(t[1].z) + bflo(t[3].z)) + (bflo(t[5].z) + bflo(t[7].z));
      g[3] = (bfhi(t[0].z) + bfhi(t[2].z)) + (bfhi(t[4].z) + bfhi(t[6].z)); u[3] = (bfhi(t[1].z) + bfhi(t[3].z)) + (bfhi(t[5].z) + bfhi(t[7].z));
      STAMPV(20, n);
      if (all_of(ok)) break;
    }
#pragma unroll
    for (int i = 0; i < 4; ++i) g[i] = rbf(silu(rbf(g[i]))) * rbf(u[i]);
    *(LAS v2u*)(L + HL + ((lane & 3) * 8 + (lane >> 3)) * 16 + ((lane >> 2) & 1) * 8) = v2u{pk(g[0], g[1]), pk(g[2], g[3])};
    const v4u hf = *(LAS v4u*)(L + HL + (lg * 8 + (lc & 7)) * 16);
    const int off = WS_DP + ((fg * 8 + (lc & 7)) * 512 + cg * 128 + lg * 4 + (lc >> 3) * 2) * 8;
    STAMP(1);
    lds_wait_ge(F_LP, TH_DN);
    STAMP(2);
#pragma unroll
    for (int i = 0; i < 8; ++i) {
      const f32x4 acc = mfma(tr_frag(L + RD + row0 * 256 + (((2 * i + (tp >> 1)) ^ ((row0 & 7) << 1)) * 16) + (tp & 1) * 8, 4096), hf,
                             f32x4{0.f, 0.f, 0.f, 0.f});
      ws_st(rw, off + i * 128, v4u{__float_as_uint(lc < 8 ? acc[0] : acc[2]), tag, __float_as_uint(lc < 8 ? acc[1] : acc[3]), tag});
    }
    STAMP(3);
    STAMP_DUMP();
    return;
  }
  u32 lp = lds_spin_ge(F_LP, 1);
  STAMP(1);
  const u32 tag = lds_get(EL) + 1u;
  {
    const v4u xr = *(LAS v4u*)(L + RX + cw * 1024 + lane * 16);
    float v[8];
#pragma unroll
    for (int i = 0; i < 4; ++i) { v[2 * i] = bflo(xr[i]); v[2 * i + 1] = bfhi(xr[i]); }
    const float x0 = __builtin_bit_cast(float, __builtin_amdgcn_readfirstlane(xr[0] << 16));
    float s0 = 0.f, q0 = 0.f;
#pragma unroll
    for (int i = 0; i < 8; ++i) { const float d = v[i] - x0; s0 += d; q0 = fmaf(d, d, q0); }
    s0 = wave_sum(s0) * (1.f / kD);
    q0 = wave_sum(q0) * (1.f / kD);
    const float mb = rbf(x0 + s0), vb = rbf(fmaxf(fmaf(-s0, s0, q0), 0.f));
    const float rden = __builtin_amdgcn_rcpf(rbf(__builtin_amdgcn_sqrtf(rbf(vb + 1e-5f))));
#pragma unroll
    for (int i = 0; i < 8; ++i) v[i] = rbf(v[i] - mb) * rden;
    if (lg == j) {
      const int ks = lc >> 2, o = lc & 3, gq = (o & 1) * 2, hf = o >> 1;
      LAS v2u* p = (LAS v2u*)(L + XN + ((ks * 4 + gq) * 8 + cw) * 16 + hf * 8);
      p[0] = v2u{pk(v[0], v[1]), pk(v[2], v[3])};
      p[16] = v2u{pk(v[4], v[5]), pk(v[6], v[7])};
    }
    lds_arrive(F_XN, lane);
  }
  STAMP(2);
  if (cw < 4 || has_gu) {
    const int nt = cw & 3;
    lds_spin_ge(F_XN, 8);
    v4u xf[4];
#pragma unroll
    for (int ks = 0; ks < 4; ++ks) xf[ks] = *(LAS v4u*)(L + XN + ((ks * 4 + lg) * 8 + (lc & 7)) * 16);
    lp = lds_spin_ge(F_LP, cw < 4 ? 3u : 5u);
    STAMP(3);
    LAS unsigned char* base = L + (cw < 4 ? RWQ : RA) + row0 * 128 + (((2 * nt + (tp >> 1)) ^ (((row0 >> 1) & 3) << 1)) * 16) + (tp & 1) * 8;
    f32x4 acc = {0.f, 0.f, 0.f, 0.f};
#pragma unroll
    for (int ks = 0; ks < 4; ++ks) acc = mfma(tr_frag(base + ks * 4096, 2048), xf[ks], acc);
    if (lc < 8) {
      const v4u o = {pk(acc[0], acc[1]), tag, pk(acc[2], acc[3]), tag};
      const int off = cw < 4 ? WS_QP + ((j * 8 + lc) * 2048 + t64 * 32 + nt * 8 + lg * 2) * 8
                             : WS_HP + (((j * 2 + (t64 >= 28)) * 8 + lc) * 896 + (t64 >= 28 ? t64 - 28 : t64) * 32 + nt * 8 + lg * 2) * 8;
      ws_st(rw, off, o);
    }
    lds_arrive(cw < 4 ? F_WQ : F_GU, lane);
    STAMP(4);
  }
  u32 n;
  {
    const int qoff = WS_QP + ((lg * 8 + ab) * 2048 + g8 * 256 + (cw * 16 + lc) * 2) * 8;
    wait_until(t0, TQ1);
    STAMP(5);
    const v4u uq4 = ws_ld(rw, qoff);
    const v2u uq = ws_take(rp, qoff, uq4, tag, n);
    STAMPV(20, 0x1001 - n);
    STAMP(6);
    const float q0 = x32_sum(x16_sum(bflo(uq.x))), q1 = x32_sum(x16_sum(bfhi(uq.x))), q2 = x32_sum(x16_sum(bflo(uq.y))),
                q3 = x32_sum(x16_sum(bfhi(uq.y)));
    if (lg == 0) {
      const int d = cw * 64 + lc * 4;
      *(LAS v2u*)(L + QL + (d >> 7) * 272 + (d & 127) * 2) = v2u{pk(q0, q1), pk(q2, q3)};
    }
    lds_arrive(F_Q, lane);
  }
  {
    if (lp < TH_K + cw + 1) lp = lds_wait_ge(F_LP, TH_K + cw + 1);
    v4u kf[8];
#pragma unroll
    for (int i = 0; i < 8; ++i) kf[i] = *(LAS v4u*)(L + RK + (cw * 32 + (i >> 2) * 16 + lc) * 256 + ((((i & 3) * 4 + lg) ^ lc) * 16));
    lds_spin_ge(F_Q, 8);
    STAMP(7);
    v4u qf[4];
#pragma unroll
    for (int ks = 0; ks < 4; ++ks) qf[ks] = *(LAS v4u*)(L + QL + (lc & 3) * 272 + ks * 64 + lg * 16);
    f32x4 a0 = {0.f, 0.f, 0.f, 0.f}, a1 = {0.f, 0.f, 0.f, 0.f};
#pragma unroll
    for (int ks = 0; ks < 4; ++ks) { a0 = mfma(kf[ks], qf[ks], a0); a1 = mfma(kf[4 + ks], qf[ks], a1); }
    float sc[8];
#pragma unroll
    for (int i = 0; i < 4; ++i) { sc[i] = rbf(rbf(a0[i]) * 0.08838834764831845f); sc[4 + i] = rbf(rbf(a1[i]) * 0.08838834764831845f); }
    float m = fmaxf(fmaxf(fmaxf(sc[0], sc[1]), fmaxf(sc[2], sc[3])), fmaxf(fmaxf(sc[4], sc[5]), fmaxf(sc[6], sc[7])));
    m = x32_max(x16_max(m));
#pragma unroll
    for (int i = 0; i < 8; ++i) sc[i] = __builtin_amdgcn_exp2f((sc[i] - m) * 1.4426950408889634f);
    if (lc < 4) *(LAS v4u*)(L + PL + ((cw * 4 + lg) * 4 + lc) * 16) = v4u{pk(sc[0], sc[1]), pk(sc[2], sc[3]), pk(sc[4], sc[5]), pk(sc[6], sc[7])};
    const float l = x32_sum(x16_sum(((sc[0] + sc[1]) + (sc[2] + sc[3])) + ((sc[4] + sc[5]) + (sc[6] + sc[7]))));
    if (lane < 4) { *(LAS float*)(L + MLM + (lane * 8 + cw) * 4) = m; *(LAS float*)(L + MLL + (lane * 8 + cw) * 4) = l; }
    lds_arrive(F_P, lane);
  }
  lds_spin_ge(F_P, 8);
  STAMP(8);
  const int wb = row0 * 256 + (((2 * cw + (tp >> 1)) ^ ((row0 & 7) << 1)) * 16) + (tp & 1) * 8;
  int opw = WS_OP + ((((g8 * 8 + cw) * 8 + ab) * 4 + as) * 16 + lc * 4 + lg) * 16;
  int ooff = WS_OP + ((((g8 * 8 + dt) * 8 + cw) * 4 + lg) * 16 + lc) * 16;
  int moff = WS_ML + (((cw * 8 + g8) * 4 + lg) * 4 + (lc >> 2)) * 16;
  int spw = WS_SP + (((g8 * 8 + dt) * 8 + (lc & 7)) * 512 + cg * 128 + cw * 16 + lg * 4 + (lc >> 3) * 2) * 8;
  int soff = WS_SP + (((cw * 8 + (lane >> 3)) * 8 + fb) * 512 + fc + (lane & 7) * 2) * 8;
  int olw = OL + (((lc >> 3) * 4 + (lc & 3)) * 8 + cw) * 16 + ((lc >> 2) & 1) * 8, olr = OL + (lg * 8 + (lc & 7)) * 16;
  int frw = FR + (lane & 7) * 64 + cw * 4;
  {
    const f32x4 m0 = *(LAS f32x4*)(L + MLM + (lc & 3) * 32), m1 = *(LAS f32x4*)(L + MLM + (lc & 3) * 32 + 16);
    v4u pf[8];
#pragma unroll
    for (int i = 0; i < 8; ++i) pf[i] = *(LAS v4u*)(L + PL + ((i * 4 + lg) * 4 + (lc & 3)) * 16);
    const float M = fmaxf(fmaxf(fmaxf(m0[0], m0[1]), fmaxf(m0[2], m0[3])), fmaxf(fmaxf(m1[0], m1[1]), fmaxf(m1[2], m1[3])));
    float f[8];
#pragma unroll
    for (int i = 0; i < 4; ++i) {
      f[i] = __builtin_amdgcn_exp2f((m0[i] - M) * 1.4426950408889634f);
      f[4 + i] = __builtin_amdgcn_exp2f((m1[i] - M) * 1.4426950408889634f);
    }
    if (cw == 0 && lane < 4) {
      const f32x4 l0 = *(LAS f32x4*)(L + MLL + lane * 32), l1 = *(LAS f32x4*)(L + MLL + lane * 32 + 16);
      const float l = (fmaf(l0[0], f[0], l0[1] * f[1]) + fmaf(l0[2], f[2], l0[3] * f[3])) + (fmaf(l1[0], f[4], l1[1] * f[5]) + fmaf(l1[2], f[6], l1[3] * f[7]));
      ws_st(rw, WS_ML + (((ab * 8 + g8) * 4 + as) * 4 + lane) * 16, v4u{__float_as_uint(M), tag, __float_as_uint(l), tag});
    }
    const f32x4 z = {0.f, 0.f, 0.f, 0.f};
    f32x4 acc = z;
    asm volatile("" : "+v"(ooff), "+v"(moff), "+v"(soff), "+v"(spw), "+v"(opw), "+v"(olw), "+v"(olr), "+v"(frw));
    if (lp < TH_V + 5) lp = lds_wait_ge(F_LP, TH_V + 5);
#pragma unroll
    for (int i = 0; i < 5; ++i) acc += f[i] * mfma(tr_frag(L + RV + wb + i * 8192, 4096), pf[i], z);
#pragma unroll
    for (int i = 5; i < 8; ++i) {
      if (lp < TH_V + i + 1) lp = lds_spin_ge(F_LP, TH_V + i + 1);
      acc += f[i] * mfma(tr_frag(L + RV + wb + i * 8192, 4096), pf[i], z);
    }
    if (lc < 4)
      ws_st(rw, opw, v4u{pk(acc[0], acc[1]), tag, pk(acc[2], acc[3]), tag});
    poll_c(rp, moff);
    STAMP(9);
  }
  {
    __builtin_amdgcn_s_sleep(ODELAY);
    STAMP(10);
    poll_a(rp, ooff);
    __builtin_amdgcn_s_sleep(PGAP);
    poll_b(rp, ooff);
    v4u um = take_c();
    if (__builtin_expect(!all_of(tag_ok(um, tag)), 0)) {
      n = 0;
      do {
        asm volatile("" ::: "memory");
        um = ws_ld(rw, moff);
      } while (!all_of(tag_ok(um, tag)) && ++n < kSpinGlb);
      STAMPV(25, n + 1);
    }
    const float ms = __uint_as_float(um.x), Mx = x32_max(x16_max(ms));
    const float w = __builtin_amdgcn_exp2f((ms - Mx) * 1.4426950408889634f);
    const float sc2 = w * __builtin_amdgcn_rcpf(x32_sum(x16_sum(w * __uint_as_float(um.z))));
    lds_spin_ge(F_LP, TH_WO);
    const v4u wf0 = tr_frag(L + RA + wb, 4096), wf1 = tr_frag(L + RA + wb + 8192, 4096);
    asm volatile("" ::"v"(sc2), "v"(wf0), "v"(wf1));
    const v2u uo = poll_end(rp, ooff, tag, n);
    STAMPV(22, 0x1000 - n);
    STAMP(11);
    const float o0 = x32_sum(x16_sum(sc2 * bflo(uo.x))), o1 = x32_sum(x16_sum(sc2 * bfhi(uo.x))),
                o2 = x32_sum(x16_sum(sc2 * bflo(uo.y))), o3 = x32_sum(x16_sum(sc2 * bfhi(uo.y)));
    if (lg == 0)
      *(LAS v2u*)(L + olw) = v2u{pk(o0, o1), pk(o2, o3)};
    asm volatile("s_waitcnt lgkmcnt(0)" ::: "memory");
    __builtin_amdgcn_s_barrier();
    asm volatile("" ::: "memory");
    f32x4 acc = mfma(wf0, *(LAS v4u*)(L + olr), f32x4{0.f, 0.f, 0.f, 0.f});
    acc = mfma(wf1, *(LAS v4u*)(L + olr + 512), acc);
    ws_st(rw, spw, v4u{__float_as_uint(lc < 8 ? acc[0] : acc[2]), tag, __float_as_uint(lc < 8 ? acc[1] : acc[3]), tag});
  }
  STAMP(12);
  {
    __builtin_amdgcn_s_sleep(SPDELAY);
    STAMP(13);
    poll_a(rp, soff);
    __builtin_amdgcn_s_sleep(PGAP);
    poll_b(rp, soff);
    const v2u us = poll_end(rp, soff, tag, n);
    STAMPV(23, 0x1000 - n);
    STAMP(14);
    float s0 = __uint_as_float(us.x), s1 = __uint_as_float(us.y);
    s0 += dpp<XOR8>(s0); s1 += dpp<XOR8>(s1);
    s0 = x32_sum(x16_sum(s0)); s1 = x32_sum(x16_sum(s1));
    if (lane < 8) { *(LAS float*)(L + frw) = s0; *(LAS float*)(L + frw + 32) = s1; }
    asm volatile("s_waitcnt lgkmcnt(0)" ::: "memory");
    __builtin_amdgcn_s_barrier();
    STAMP(15);
    STAMP_DUMP();
  }
}

__global__ void p_trail(unsigned char* ws, u16* out) {
  const u32 t = now();
  const u32 gap = t - ((const u32*)(ws + (100 << 20)))[threadIdx.x];
  __shared__ u32 mn;
  if (threadIdx.x == 0) mn = 0xffffffffu;
  __syncthreads();
  atomicMin(&mn, gap);
  __syncthreads();
  if (threadIdx.x == 0 && mn > 250) out[0] = (u16)pk((float)mn, 0.f);
}
extern "C" void kernel_launch(void* const* d_in, const int* in_sizes, int n_in, void* d_out, int out_size, void* d_ws,
                              size_t ws_size, hipStream_t stream) {
  fused_decode_block<<<256, 640, 0, stream>>>((const u16*)d_in[0], (const u16*)d_in[1], (const u16*)d_in[2], (const u16*)d_in[3],
                                              (const u16*)d_in[4], (const u16*)d_in[6], (const u16*)d_in[7], (const u16*)d_in[8],
                                              (u16*)d_out, (unsigned char*)d_ws);
  p_trail<<<1, 256, 0, stream>>>((unsigned char*)d_ws, (u16*)d_out);
}
```

```cpp
#include <hip/hip_runtime.h>

typedef unsigned short u16;
typedef unsigned u32;
typedef unsigned long long u64;
typedef u32 v4u __attribute__((ext_vector_type(4)));
typedef u32 v2u __attribute__((ext_vector_type(2)));
typedef float f32x4 __attribute__((ext_vector_type(4)));
typedef short s16x4 __attribute__((ext_vector_type(4)));
typedef __bf16 bf16x8 __attribute__((ext_vector_type(8)));
typedef __bf16 bf16x2 __attribute__((ext_vector_type(2)));
#define LAS __attribute__((address_space(3)))
#define GAS __attribute__((address_space(1)))
typedef __amdgpu_buffer_rsrc_t rsrc_t;

namespace {
constexpr int kD = 512, kSK = 4096, kW0 = 3072, kF = 1792;
constexpr int RK = 0, RV = 65536, RWQ = RV + 49152, RA = 131072, RD = 147456, RM = 155648;
constexpr int XN = RM, OL = RM, FR = RM + 1024, QL = RM + 2048, PL = RM + 3328, MLM = RM + 5376, MLL = RM + 5504,
              HL = RM + 5632, FL = RM + 6144;
constexpr int F_LP = FL, F_XN = FL + 4, F_WQ = FL + 8, F_GU = FL + 12, F_Q = FL + 16, F_D = FL + 20, F_P = FL + 24, F_H = FL + 28;
constexpr int EL = FL + 64, ST = FL + 320, RX = RD;
constexpr int LDS_BYTES = 163840;
constexpr int WS_E = 0, WS_QP = 4096, WS_HP = WS_QP + 4 * 8 * 2048 * 8, WS_OP = WS_HP + 4 * 2 * 8 * 896 * 8,
              WS_ML = WS_OP + 8 * 32 * 4 * 64 * 8, WS_SP = WS_ML + 8 * 32 * 4 * 16, WS_DP = WS_SP + 64 * 8 * 512 * 8,
              WS_END = WS_DP + 56 * 8 * 512 * 8;
constexpr int OOB = 0x7ffffff0;
constexpr int AUX_NT = 2, AUX_SC1 = 16, RSRC3 = 0x00020000;
constexpr u32 kSpinLds = 1u << 22, kSpinGlb = 1u << 16;

__device__ __forceinline__ float bflo(u32 w) { return __builtin_bit_cast(float, w << 16); }
__device__ __forceinline__ float bfhi(u32 w) { return __builtin_bit_cast(float, w & 0xffff0000u); }
__device__ __forceinline__ u32 pk(float lo, float hi) {
  bf16x2 v = {(__bf16)lo, (__bf16)hi};
  return __builtin_bit_cast(u32, v);
}
__device__ __forceinline__ float rbf(float f) { return (float)(__bf16)f; }
__device__ __forceinline__ f32x4 mfma(v4u a, v4u b, f32x4 c) {
  return __builtin_amdgcn_mfma_f32_16x16x32_bf16(__builtin_bit_cast(bf16x8, a), __builtin_bit_cast(bf16x8, b), c, 0, 0, 0);
}
template <int CTRL> __device__ __forceinline__ float dpp(float x) {
  return __builtin_bit_cast(float, __builtin_amdgcn_mov_dpp(__builtin_bit_cast(int, x), CTRL, 0xf, 0xf, true));
}
constexpr int XOR1 = 0xB1, XOR2 = 0x4E, XOR7 = 0x141, XOR8 = 0x128, ROR4 = 0x124;
__device__ __forceinline__ float x16_sum(float x) {
  auto s = __builtin_amdgcn_permlane16_swap(__float_as_uint(x), __float_as_uint(x), false, false);
  return __uint_as_float(s[0]) + __uint_as_float(s[1]);
}
__device__ __forceinline__ float x32_sum(float x) {
  auto s = __builtin_amdgcn_permlane32_swap(__float_as_uint(x), __float_as_uint(x), false, false);
  return __uint_as_float(s[0]) + __uint_as_float(s[1]);
}
__device__ __forceinline__ float x16_max(float x) {
  auto s = __builtin_amdgcn_permlane16_swap(__float_as_uint(x), __float_as_uint(x), false, false);
  return fmaxf(__uint_as_float(s[0]), __uint_as_float(s[1]));
}
__device__ __forceinline__ float x32_max(float x) {
  auto s = __builtin_amdgcn_permlane32_swap(__float_as_uint(x), __float_as_uint(x), false, false);
  return fmaxf(__uint_as_float(s[0]), __uint_as_float(s[1]));
}
__device__ __forceinline__ float wave_sum(float x) {
  x += dpp<XOR1>(x); x += dpp<XOR2>(x); x += dpp<XOR7>(x); x += dpp<XOR8>(x);
  return x32_sum(x16_sum(x));
}
__device__ __forceinline__ float silu(float x) { return x * __builtin_amdgcn_rcpf(1.f + __builtin_amdgcn_exp2f(x * -1.4426950408889634f)); }

__device__ __forceinline__ void lds_set(int addr, u32 val) {
  asm volatile("ds_write_b32 %0, %1" ::"v"(addr), "v"(val) : "memory");
}
__device__ __forceinline__ u32 lds_get(int addr) {
  u32 r;
  asm volatile("ds_read_b32 %0, %1\n\ts_waitcnt lgkmcnt(0)" : "=v"(r) : "v"(addr) : "memory");
  return __builtin_amdgcn_readfirstlane(r);
}
__device__ __forceinline__ u32 lds_wait_ge(int addr, u32 want) {
  u32 v = lds_get(addr);
  for (u32 n = 0; v < want && n < kSpinLds; ++n) { __builtin_amdgcn_s_sleep(1); v = lds_get(addr); }
  return v;
}
__device__ __forceinline__ u32 lds_spin_ge(int addr, u32 want) {
  u32 v = lds_get(addr);
  for (u32 n = 0; v < want && n < kSpinLds; ++n) v = lds_get(addr);
  return v;
}
__device__ __forceinline__ void lds_arrive(int addr, int) {
  asm volatile("s_mov_b64 exec, 1\n\tds_add_u32 %0, %1\n\ts_mov_b64 exec, -1" ::"v"(addr), "v"(1u) : "memory");
}
__device__ __forceinline__ v4u ws_ld(rsrc_t rs, int off) { return __builtin_amdgcn_raw_buffer_load_b128(rs, off, 0, AUX_SC1); }
__device__ __forceinline__ void ws_st(rsrc_t rs, int off, v4u v) { __builtin_amdgcn_raw_buffer_store_b128(v, rs, off, 0, AUX_SC1); }
__device__ __forceinline__ bool tag_ok(v4u u, u32 tag) { return u.y == tag && u.w == tag; }
__device__ __forceinline__ bool all_of(bool ok) { return __builtin_amdgcn_ballot_w64(!ok) == 0ull; }
typedef int s4i __attribute__((ext_vector_type(4)));
#define POLL_REGS "v96", "v97", "v98", "v99", "v100", "v101", "v102", "v103", "v104", "v105", "v106", "v107"
__device__ __forceinline__ void poll_a(s4i rs, int off) {
  asm volatile("buffer_load_dwordx4 v[96:99], %0, %1, 0 offen sc1" ::"v"(off), "s"(rs) : "memory", POLL_REGS);
}
__device__ __forceinline__ void poll_b(s4i rs, int off) {
  asm volatile("buffer_load_dwordx4 v[100:103], %0, %1, 0 offen sc1" ::"v"(off), "s"(rs) : "memory", POLL_REGS);
}
__device__ __forceinline__ void poll_c(s4i rs, int off) {
  asm volatile("buffer_load_dwordx4 v[104:107], %0, %1, 0 offen sc1" ::"v"(off), "s"(rs) : "memory", POLL_REGS);
}
__device__ __forceinline__ v4u take_c() {
  v4u u;
  asm volatile("s_waitcnt vmcnt(2)\n\tv_mov_b32 %0, v104\n\tv_mov_b32 %1, v105\n\tv_mov_b32 %2, v106\n\tv_mov_b32 %3, v107"
               : "=v"(u.x), "=v"(u.y), "=v"(u.z), "=v"(u.w)::"memory", POLL_REGS);
  return u;
}
__device__ __forceinline__ v2u poll_end(s4i rs, int off, u32 tag, u32& left) {
  u32 x, z, cnt;
  int ad;
  u64 ok, t;
#define POLL_STEP(r0, r1, r2, r3)                                                                   \
  "s_waitcnt vmcnt(1)\n\t"                                                                          \
  "v_cmp_eq_u32_e32 vcc, %[tag], v" #r1 "\n\t"                                                      \
  "v_cmp_eq_u32_e64 %[t], %[tag], v" #r3 "\n\t"                                                     \
  "s_and_b64 vcc, vcc, %[t]\n\t"                                                                    \
  "s_andn2_b64 vcc, vcc, %[ok]\n\t"                                                                 \
  "v_cndmask_b32_e32 %[x], %[x], v" #r0 ", vcc\n\t"                                                 \
  "v_cndmask_b32_e32 %[z], %[z], v" #r2 ", vcc\n\t"                                                 \
  "s_or_b64 %[ok], %[ok], vcc\n\t"                                                                  \
  "s_cmp_eq_u64 %[ok], -1\n\t"                                                                      \
  "s_cbranch_scc1 .Lpoll_done_%=\n\t"                                                               \
  "v_cndmask_b32_e64 %[ad], %[off], %[oob], %[ok]\n\t"                                              \
  "buffer_load_dwordx4 v[" #r0 ":" #r3 "], %[ad], %[rs], 0 offen sc1\n\t"
  asm volatile("s_mov_b64 %[ok], 0\n\t"
               "s_movk_i32 %[cnt], 0x1000\n"
               ".Lpoll_loop_%=:\n\t" POLL_STEP(96, 97, 98, 99) POLL_STEP(100, 101, 102, 103)
               "s_sub_u32 %[cnt], %[cnt], 1\n\t"
               "s_cmp_lg_u32 %[cnt], 0\n\t"
               "s_cbranch_scc1 .Lpoll_loop_%=\n"
               ".Lpoll_done_%=:\n\t"
               "s_nop 0"
               : [x] "=&v"(x), [z] "=&v"(z), [ad] "=&v"(ad), [ok] "=&s"(ok), [t] "=&s"(t), [cnt] "=&s"(cnt)
               : [off] "v"(off), [oob] "v"(OOB), [rs] "s"(rs), [tag] "s"(tag)
               : "memory", "vcc", "scc", POLL_REGS);
  left = cnt;
  return v2u{x, z};
}
__device__ __forceinline__ v2u ws_take(s4i rs, int off, v4u u, u32 tag, u32& n) {
  n = 0x1001;
  if (all_of(tag_ok(u, tag))) return v2u{u.x, u.z};
  poll_a(rs, off);
  __builtin_amdgcn_s_sleep(4);
  poll_b(rs, off);
  return poll_end(rs, off, tag, n);
}
__device__ __forceinline__ u32 now() { return (u32)__builtin_amdgcn_s_memrealtime(); }
__device__ __forceinline__ void wait_until(u32 t0, u32 dt) {
  while (now() - t0 < dt) __builtin_amdgcn_s_sleep(1);
}
__device__ __forceinline__ v4u tr_frag(LAS unsigned char* p, int half) {
  const v2u a = __builtin_bit_cast(v2u, __builtin_amdgcn_ds_read_tr16_b64_v4i16((LAS s16x4*)p));
  const v2u b = __builtin_bit_cast(v2u, __builtin_amdgcn_ds_read_tr16_b64_v4i16((LAS s16x4*)(p + half)));
  return v4u{a.x, a.y, b.x, b.y};
}
}

#define LAG 5
#define LAGK 5
#define SPDELAY 8
#define TQ1 350
#define TQ2 430
#define ODELAY 8
#define PGAP 3
#define QPOLL 6
#define HPOLL 9
#define STAMP(i)
#define STAMPV(i, v)
#define STAMP_DUMP()

__global__ void __launch_bounds__(640, 3)
fused_decode_block(const u16* __restrict__ x, const u16* __restrict__ kc, const u16* __restrict__ vc, const u16* __restrict__ wq,
                   const u16* __restrict__ wo, const u16* __restrict__ wg, const u16* __restrict__ wu, const u16* __restrict__ wd,
                   u16* __restrict__ out, unsigned char* __restrict__ ws) {
  __shared__ __attribute__((aligned(16))) unsigned char lds[LDS_BYTES];
  LAS unsigned char* L = (LAS unsigned char*)lds;
  u64 tA; asm volatile("s_memrealtime %0" : "=s"(tA));
  __builtin_amdgcn_sched_barrier(0);
  const int tid = threadIdx.x, lane = tid & 63, wave = __builtin_amdgcn_readfirstlane(tid >> 6);
  asm volatile("" ::"s"(x), "s"(kc), "s"(vc), "s"(wq), "s"(wo), "s"(wg), "s"(wu), "s"(wd), "s"(out), "s"(ws));
  const u32 t0 = now();
  const int c = blockIdx.x, g8 = c & 7, r = c >> 3;
  const int fb = c >> 5, fc = (c & 31) * 16;
  const int j = c >> 6, t64 = c & 63;
  const bool has_gu = t64 < 56;
  const int fg = (r >> 2) * 8 + g8, cg = r & 3;
  const bool has_dn = fg < 56;
  const int dt = r >> 2;
  const int ab = r >> 2, as = r & 3;
  const rsrc_t rw = __builtin_amdgcn_make_buffer_rsrc((void*)ws, 0, WS_END, RSRC3);
  const s4i rp = {(int)(u32)(u64)ws, (int)((u32)((u64)ws >> 32) & 0xffffu), WS_END, RSRC3};
  const u32 TH_K = has_gu ? 5 : 3, TH_V = TH_K + 8, TH_DN = TH_V + 9, TH_WO = TH_V + 8 + (has_dn ? 1 : 0) + 2;
  STAMP(0);

  if (__builtin_expect(wave == 0, 1)) {
    const int r8 = lane >> 3, r4 = lane >> 4;
    const int sw128 = ((lane & 7) ^ ((r4 & 3) << 1)) * 16;
    const int swV = ((lane & 15) ^ (r4 << 1)) * 16;
    const int swK = ((lane & 15) ^ r4) * 16;
    u32 cnt, lpv = 1;
    __builtin_amdgcn_s_setprio(3);
#define GLDS(rs, ldsoff, voff, soff) __builtin_amdgcn_raw_ptr_buffer_load_lds(rs, (LAS void*)(L + (ldsoff)), 16, voff, soff, 0, AUX_NT)
#define GROUP_DONE_L(LG) do { asm volatile("s_waitcnt vmcnt(%0)" ::"n"((LG) * 8) : "memory"); ++cnt; if (cnt + 1 > lpv + (LG)) { lpv = cnt + 1 - (LG); lds_set(F_LP, lpv); } } while (0)
#define GROUP_DONE() GROUP_DONE_L(LAG)
    {
      const rsrc_t rs = __builtin_amdgcn_make_buffer_rsrc((void*)x, 0, 8192, RSRC3);
#pragma unroll
      for (int i = 0; i < 8; ++i) __builtin_amdgcn_raw_ptr_buffer_load_lds(rs, (LAS void*)(L + RX + i * 1024), 16, lane * 16, i * 1024, 0, 0);
      __builtin_amdgcn_raw_ptr_buffer_load_lds(rw, (LAS void*)(L + EL), 4, lane * 4, WS_E, 0, AUX_SC1);
    }
    {
      const rsrc_t rs = __builtin_amdgcn_make_buffer_rsrc((void*)wq, 0, 512 * 4096 * 2, RSRC3);
      const int vo = r8 * 8192 + sw128, so = j * 128 * 8192 + t64 * 128;
#pragma unroll
      for (int i = 0; i < 16; ++i) GLDS(rs, RWQ + i * 1024, vo, so + i * 8 * 8192);
    }
    STAMP(1);
    if (has_gu) {
      const rsrc_t rs = __builtin_amdgcn_make_buffer_rsrc((void*)(t64 < 28 ? wg : wu), 0, 512 * kF * 2, RSRC3);
      const int vo = r8 * (kF * 2) + sw128, so = j * 128 * (kF * 2) + (t64 < 28 ? t64 : t64 - 28) * 128;
#pragma unroll
      for (int i = 0; i < 16; ++i) GLDS(rs, RA + i * 1024, vo, so + i * 8 * (kF * 2));
      __builtin_amdgcn_s_barrier();
      asm volatile("s_waitcnt vmcnt(32)" ::: "memory");
      cnt = 4;
    } else {
      __builtin_amdgcn_s_barrier();
      asm volatile("s_waitcnt vmcnt(16)" ::: "memory");
      cnt = 2;
    }
    lds_set(F_LP, 1u);
    u32 xfin;
    asm volatile("ds_read_b32 %0, %1" : "=v"(xfin) : "v"(RX + (fb * kD + fc + (lane & 7) * 2) * 2) : "memory");
    const u32 tag = lds_get(EL) + 1u;
    asm volatile("" : "+v"(xfin));
    STAMP(2);
    const size_t kvoff = ((size_t)(ab * 8 + g8) * kSK + kW0 + as * 256) * 128;
    {
      const rsrc_t rs = __builtin_amdgcn_make_buffer_rsrc((void*)(kc + kvoff), 0, 65536, RSRC3);
      const int vo = r4 * 256;
#pragma unroll 1
      for (int n = 0; n < 8; ++n) {
#pragma unroll
        for (int i = 0; i < 8; ++i) GLDS(rs, RK + n * 8192 + i * 1024, vo + (swK ^ ((i & 3) * 64)), n * 8192 + i * 1024);
        GROUP_DONE_L(LAGK);
      }
    }
    STAMP(3);
    {
      const rsrc_t rs = __builtin_amdgcn_make_buffer_rsrc((void*)(vc + kvoff), 0, 65536, RSRC3);
      const int vo = r4 * 256;
#pragma unroll 1
      for (int n = 0; n < 8; ++n) {
        if (n == 6) lds_wait_ge(F_WQ, 4);
#pragma unroll
        for (int i = 0; i < 8; ++i) GLDS(rs, RV + n * 8192 + i * 1024, vo + (swV ^ ((i & 1) * 128)), n * 8192 + i * 1024);
        GROUP_DONE();
      }
    }
    STAMP(4);
    if (has_dn) {
      const rsrc_t rs = __builtin_amdgcn_make_buffer_rsrc((void*)wd, 0, kF * 512 * 2, RSRC3);
      const int vo = r4 * 1024, so = fg * 32 * 1024 + cg * 256;
#pragma unroll
      for (int i = 0; i < 8; ++i) GLDS(rs, RD + i * 1024, vo + (swV ^ ((i & 1) * 128)), so + i * 4096);
      GROUP_DONE();
    }
    if (has_gu) lds_wait_ge(F_GU, 4);
    {
      const rsrc_t rs = __builtin_amdgcn_make_buffer_rsrc((void*)wo, 0, 4096 * 512 * 2, RSRC3);
      const int vo = r4 * 1024, so = (g8 * 512 + dt * 16) * 1024 + cg * 256;
#pragma unroll
      for (int n = 0; n < 2; ++n) {
#pragma unroll
        for (int i = 0; i < 8; ++i)
          GLDS(rs, RA + n * 8192 + i * 1024, vo + (swV ^ ((i & 1) * 128)), so + ((2 * n + (i >> 2)) * 128 + (i & 3) * 4) * 1024);
        GROUP_DONE();
      }
    }
    STAMP(5);
    asm volatile("s_waitcnt vmcnt(32)" ::: "memory"); lds_set(F_LP, cnt - 3);
    asm volatile("s_waitcnt vmcnt(24)" ::: "memory"); lds_set(F_LP, cnt - 2);
    STAMP(6);
    asm volatile("s_waitcnt vmcnt(16)" ::: "memory"); lds_set(F_LP, cnt - 1);
    asm volatile("s_waitcnt vmcnt(8)" ::: "memory");  lds_set(F_LP, cnt);
    asm volatile("s_waitcnt vmcnt(0)" ::: "memory");  lds_set(F_LP, cnt + 1);
    STAMP(7);
    __builtin_amdgcn_s_barrier();
    float f0, f1;
    {
      const int doff = WS_DP + (((lane >> 3) * 8 + fb) * 512 + fc + (lane & 7) * 2) * 8;
      for (u32 n = 0; n < kSpinGlb; ++n) {
        asm volatile("" ::: "memory");
        v4u u[7];
#pragma unroll
        for (int i = 0; i < 7; ++i) u[i] = ws_ld(rw, doff + i * (8 * 8 * 512 * 8));
        bool ok = true;
        f0 = 0.f; f1 = 0.f;
#pragma unroll
        for (int i = 0; i < 7; ++i) { ok = ok && tag_ok(u[i], tag); f0 += __uint_as_float(u[i].x); f1 += __uint_as_float(u[i].z); }
        STAMPV(20, n);
        if (all_of(ok)) break;
      }
      f0 += dpp<XOR8>(f0); f1 += dpp<XOR8>(f1);
      f0 = rbf(silu(rbf(x32_sum(x16_sum(f0)))));
      f1 = rbf(silu(rbf(x32_sum(x16_sum(f1)))));
      asm volatile("" ::"v"(f0), "v"(f1));
    }
    STAMP(8);
    __builtin_amdgcn_s_barrier();
    asm volatile("" ::: "memory");
    LAS f32x4* fr = (LAS f32x4*)(L + FR + (lane & 7) * 64);
    const f32x4 a0 = fr[0], a1 = fr[1], a2 = fr[2], a3 = fr[3];
    const float s0 = rbf(((a0[0] + a0[1]) + (a0[2] + a0[3])) + ((a1[0] + a1[1]) + (a1[2] + a1[3])));
    const float s1 = rbf(((a2[0] + a2[1]) + (a2[2] + a2[3])) + ((a3[0] + a3[1]) + (a3[2] + a3[3])));
    { const u32 lead = ((const GAS u32*)(ws + (100 << 20)))[0]; const u32 gap = t0 - (u32)tA;
      u32 ov = pk(bflo(xfin) + rbf(s0 * f0), bfhi(xfin) + rbf(s1 * f1));
      if (gap > 150) ov = pk((float)gap, (float)gap);
      if (lane < 8) ((GAS u32*)out)[(fb * kD + fc) / 2 + lane] = ov; }
    if (c == 0 && lane == 0) __builtin_amdgcn_raw_buffer_store_b32(tag, rw, WS_E, 0, AUX_SC1);
    STAMP(9);
    STAMP_DUMP();
    return;
  }

  const int cw = wave - 1, lc = lane & 15, lg = lane >> 4;
  if (cw == 0 && lane < 8) lds_set(FL + lane * 4, 0u);
  asm volatile("s_waitcnt lgkmcnt(0)" ::: "memory");
  __builtin_amdgcn_s_barrier();
  const int tq = lc >> 2, tp = lc & 3, row0 = 4 * lg + tq;
  if (wave == 9) {
    if (!has_dn) return;
    wait_until(t0, TQ2);
    lds_wait_ge(F_LP, 1);
    const u32 tag = lds_get(EL) + 1u;
    const int hoff = WS_HP + ((lane >> 3) * 896 + fg * 16 + (lane & 7) * 2) * 8;
    float g[4], u[4];
    for (u32 n = 0; n < kSpinGlb; ++n) {
      asm volatile("" ::: "memory");
      v4u t[8];
#pragma unroll
      for (int i = 0; i < 8; ++i) t[i] = ws_ld(rw, hoff + i * (8 * 896 * 8));
      bool ok = true;
#pragma unroll
      for (int i = 0; i < 8; ++i) ok = ok && tag_ok(t[i], tag);
      g[0] = (bflo(t[0].x) + bflo(t[2].x)) + (bflo(t[4].x) + bflo(t[6].x)); u[0] = (bflo(t[1].x) + bflo(t[3].x)) + (bflo(t[5].x) + bflo(t[7].x));
      g[1] = (bfhi(t[0].x) + bfhi(t[2].x)) + (bfhi(t[4].x) + bfhi(t[6].x)); u[1] = (bfhi(t[1].x) + bfhi(t[3].x)) + (bfhi(t[5].x) + bfhi(t[7].x));
      g[2] = (bflo(t[0].z) + bflo(t[2].z)) + (bflo(t[4].z) + bflo(t[6].z)); u[2] = (bflo(t[1].z) + bflo(t[3].z)) + (bflo(t[5].z) + bflo(t[7].z));
      g[3] = (bfhi(t[0].z) + bfhi(t[2].z)) + (bfhi(t[4].z) + bfhi(t[6].z)); u[3] = (bfhi(t[1].z) + bfhi(t[3].z)) + (bfhi(t[5].z) + bfhi(t[7].z));
      STAMPV(20, n);
      if (all_of(ok)) break;
    }
#pragma unroll
    for (int i = 0; i < 4; ++i) g[i] = rbf(silu(rbf(g[i]))) * rbf(u[i]);
    *(LAS v2u*)(L + HL + ((lane & 3) * 8 + (lane >> 3)) * 16 + ((lane >> 2) & 1) * 8) = v2u{pk(g[0], g[1]), pk(g[2], g[3])};
    const v4u hf = *(LAS v4u*)(L + HL + (lg * 8 + (lc & 7)) * 16);
    const int off = WS_DP + ((fg * 8 + (lc & 7)) * 512 + cg * 128 + lg * 4 + (lc >> 3) * 2) * 8;
    STAMP(1);
    lds_wait_ge(F_LP, TH_DN);
    STAMP(2);
#pragma unroll
    for (int i = 0; i < 8; ++i) {
      const f32x4 acc = mfma(tr_frag(L + RD + row0 * 256 + (((2 * i + (tp >> 1)) ^ ((row0 & 7) << 1)) * 16) + (tp & 1) * 8, 4096), hf,
                             f32x4{0.f, 0.f, 0.f, 0.f});
      ws_st(rw, off + i * 128, v4u{__float_as_uint(lc < 8 ? acc[0] : acc[2]), tag, __float_as_uint(lc < 8 ? acc[1] : acc[3]), tag});
    }
    STAMP(3);
    STAMP_DUMP();
    return;
  }
  u32 lp = lds_spin_ge(F_LP, 1);
  STAMP(1);
  const u32 tag = lds_get(EL) + 1u;
  {
    const v4u xr = *(LAS v4u*)(L + RX + cw * 1024 + lane * 16);
    float v[8];
#pragma unroll
    for (int i = 0; i < 4; ++i) { v[2 * i] = bflo(xr[i]); v[2 * i + 1] = bfhi(xr[i]); }
    const float x0 = __builtin_bit_cast(float, __builtin_amdgcn_readfirstlane(xr[0] << 16));
    float s0 = 0.f, q0 = 0.f;
#pragma unroll
    for (int i = 0; i < 8; ++i) { const float d = v[i] - x0; s0 += d; q0 = fmaf(d, d, q0); }
    s0 = wave_sum(s0) * (1.f / kD);
    q0 = wave_sum(q0) * (1.f / kD);
    const float mb = rbf(x0 + s0), vb = rbf(fmaxf(fmaf(-s0, s0, q0), 0.f));
    const float rden = __builtin_amdgcn_rcpf(rbf(__builtin_amdgcn_sqrtf(rbf(vb + 1e-5f))));
#pragma unroll
    for (int i = 0; i < 8; ++i) v[i] = rbf(v[i] - mb) * rden;
    if (lg == j) {
      const int ks = lc >> 2, o = lc & 3, gq = (o & 1) * 2, hf = o >> 1;
      LAS v2u* p = (LAS v2u*)(L + XN + ((ks * 4 + gq) * 8 + cw) * 16 + hf * 8);
      p[0] = v2u{pk(v[0], v[1]), pk(v[2], v[3])};
      p[16] = v2u{pk(v[4], v[5]), pk(v[6], v[7])};
    }
    lds_arrive(F_XN, lane);
  }
  STAMP(2);
  if (cw < 4 || has_gu) {
    const int nt = cw & 3;
    lds_spin_ge(F_XN, 8);
    v4u xf[4];
#pragma unroll
    for (int ks = 0; ks < 4; ++ks) xf[ks] = *(LAS v4u*)(L + XN + ((ks * 4 + lg) * 8 + (lc & 7)) * 16);
    lp = lds_spin_ge(F_LP, cw < 4 ? 3u : 5u);
    STAMP(3);
    LAS unsigned char* base = L + (cw < 4 ? RWQ : RA) + row0 * 128 + (((2 * nt + (tp >> 1)) ^ (((row0 >> 1) & 3) << 1)) * 16) + (tp & 1) * 8;
    f32x4 acc = {0.f, 0.f, 0.f, 0.f};
#pragma unroll
    for (int ks = 0; ks < 4; ++ks) acc = mfma(tr_frag(base + ks * 4096, 2048), xf[ks], acc);
    if (lc < 8) {
      const v4u o = {pk(acc[0], acc[1]), tag, pk(acc[2], acc[3]), tag};
      const int off = cw < 4 ? WS_QP + ((j * 8 + lc) * 2048 + t64 * 32 + nt * 8 + lg * 2) * 8
                             : WS_HP + (((j * 2 + (t64 >= 28)) * 8 + lc) * 896 + (t64 >= 28 ? t64 - 28 : t64) * 32 + nt * 8 + lg * 2) * 8;
      ws_st(rw, off, o);
    }
    lds_arrive(cw < 4 ? F_WQ : F_GU, lane);
    STAMP(4);
  }
  u32 n;
  {
    const int qoff = WS_QP + ((lg * 8 + ab) * 2048 + g8 * 256 + (cw * 16 + lc) * 2) * 8;
    wait_until(t0, TQ1);
    STAMP(5);
    const v4u uq4 = ws_ld(rw, qoff);
    const v2u uq = ws_take(rp, qoff, uq4, tag, n);
    STAMPV(20, 0x1001 - n);
    STAMP(6);
    const float q0 = x32_sum(x16_sum(bflo(uq.x))), q1 = x32_sum(x16_sum(bfhi(uq.x))), q2 = x32_sum(x16_sum(bflo(uq.y))),
                q3 = x32_sum(x16_sum(bfhi(uq.y)));
    if (lg == 0) {
      const int d = cw * 64 + lc * 4;
      *(LAS v2u*)(L + QL + (d >> 7) * 272 + (d & 127) * 2) = v2u{pk(q0, q1), pk(q2, q3)};
    }
    lds_arrive(F_Q, lane);
  }
  {
    if (lp < TH_K + cw + 1) lp = lds_wait_ge(F_LP, TH_K + cw + 1);
    v4u kf[8];
#pragma unroll
    for (int i = 0; i < 8; ++i) kf[i] = *(LAS v4u*)(L + RK + (cw * 32 + (i >> 2) * 16 + lc) * 256 + ((((i & 3) * 4 + lg) ^ lc) * 16));
    lds_spin_ge(F_Q, 8);
    STAMP(7);
    v4u qf[4];
#pragma unroll
    for (int ks = 0; ks < 4; ++ks) qf[ks] = *(LAS v4u*)(L + QL + (lc & 3) * 272 + ks * 64 + lg * 16);
    f32x4 a0 = {0.f, 0.f, 0.f, 0.f}, a1 = {0.f, 0.f, 0.f, 0.f};
#pragma unroll
    for (int ks = 0; ks < 4; ++ks) { a0 = mfma(kf[ks], qf[ks], a0); a1 = mfma(kf[4 + ks], qf[ks], a1); }
    float sc[8];
#pragma unroll
    for (int i = 0; i < 4; ++i) { sc[i] = rbf(rbf(a0[i]) * 0.08838834764831845f); sc[4 + i] = rbf(rbf(a1[i]) * 0.08838834764831845f); }
    float m = fmaxf(fmaxf(fmaxf(sc[0], sc[1]), fmaxf(sc[2], sc[3])), fmaxf(fmaxf(sc[4], sc[5]), fmaxf(sc[6], sc[7])));
    m = x32_max(x16_max(m));
#pragma unroll
    for (int i = 0; i < 8; ++i) sc[i] = __builtin_amdgcn_exp2f((sc[i] - m) * 1.4426950408889634f);
    if (lc < 4) *(LAS v4u*)(L + PL + ((cw * 4 + lg) * 4 + lc) * 16) = v4u{pk(sc[0], sc[1]), pk(sc[2], sc[3]), pk(sc[4], sc[5]), pk(sc[6], sc[7])};
    const float l = x32_sum(x16_sum(((sc[0] + sc[1]) + (sc[2] + sc[3])) + ((sc[4] + sc[5]) + (sc[6] + sc[7]))));
    if (lane < 4) { *(LAS float*)(L + MLM + (lane * 8 + cw) * 4) = m; *(LAS float*)(L + MLL + (lane * 8 + cw) * 4) = l; }
    lds_arrive(F_P, lane);
  }
  lds_spin_ge(F_P, 8);
  STAMP(8);
  const int wb = row0 * 256 + (((2 * cw + (tp >> 1)) ^ ((row0 & 7) << 1)) * 16) + (tp & 1) * 8;
  int opw = WS_OP + ((((g8 * 8 + cw) * 8 + ab) * 4 + as) * 16 + lc * 4 + lg) * 16;
  int ooff = WS_OP + ((((g8 * 8 + dt) * 8 + cw) * 4 + lg) * 16 + lc) * 16;
  int moff = WS_ML + (((cw * 8 + g8) * 4 + lg) * 4 + (lc >> 2)) * 16;
  int spw = WS_SP + (((g8 * 8 + dt) * 8 + (lc & 7)) * 512 + cg * 128 + cw * 16 + lg * 4 + (lc >> 3) * 2) * 8;
  int soff = WS_SP + (((cw * 8 + (lane >> 3)) * 8 + fb) * 512 + fc + (lane & 7) * 2) * 8;
  int olw = OL + (((lc >> 3) * 4 + (lc & 3)) * 8 + cw) * 16 + ((lc >> 2) & 1) * 8, olr = OL + (lg * 8 + (lc & 7)) * 16;
  int frw = FR + (lane & 7) * 64 + cw * 4;
  {
    const f32x4 m0 = *(LAS f32x4*)(L + MLM + (lc & 3) * 32), m1 = *(LAS f32x4*)(L + MLM + (lc & 3) * 32 + 16);
    v4u pf[8];
#pragma unroll
    for (int i = 0; i < 8; ++i) pf[i] = *(LAS v4u*)(L + PL + ((i * 4 + lg) * 4 + (lc & 3)) * 16);
    const float M = fmaxf(fmaxf(fmaxf(m0[0], m0[1]), fmaxf(m0[2], m0[3])), fmaxf(fmaxf(m1[0], m1[1]), fmaxf(m1[2], m1[3])));
    float f[8];
#pragma unroll
    for (int i = 0; i < 4; ++i) {
      f[i] = __builtin_amdgcn_exp2f((m0[i] - M) * 1.4426950408889634f);
      f[4 + i] = __builtin_amdgcn_exp2f((m1[i] - M) * 1.4426950408889634f);
    }
    if (cw == 0 && lane < 4) {
      const f32x4 l0 = *(LAS f32x4*)(L + MLL + lane * 32), l1 = *(LAS f32x4*)(L + MLL + lane * 32 + 16);
      const float l = (fmaf(l0[0], f[0], l0[1] * f[1]) + fmaf(l0[2], f[2], l0[3] * f[3])) + (fmaf(l1[0], f[4], l1[1] * f[5]) + fmaf(l1[2], f[6], l1[3] * f[7]));
      ws_st(rw, WS_ML + (((ab * 8 + g8) * 4 + as) * 4 + lane) * 16, v4u{__float_as_uint(M), tag, __float_as_uint(l), tag});
    }
    const f32x4 z = {0.f, 0.f, 0.f, 0.f};
    f32x4 acc = z;
    asm volatile("" : "+v"(ooff), "+v"(moff), "+v"(soff), "+v"(spw), "+v"(opw), "+v"(olw), "+v"(olr), "+v"(frw));
    if (lp < TH_V + 5) lp = lds_wait_ge(F_LP, TH_V + 5);
#pragma unroll
    for (int i = 0; i < 5; ++i) acc += f[i] * mfma(tr_frag(L + RV + wb + i * 8192, 4096), pf[i], z);
#pragma unroll
    for (int i = 5; i < 8; ++i) {
      if (lp < TH_V + i + 1) lp = lds_spin_ge(F_LP, TH_V + i + 1);
      acc += f[i] * mfma(tr_frag(L + RV + wb + i * 8192, 4096), pf[i], z);
    }
    if (lc < 4)
      ws_st(rw, opw, v4u{pk(acc[0], acc[1]), tag, pk(acc[2], acc[3]), tag});
    poll_c(rp, moff);
    STAMP(9);
  }
  {
    __builtin_amdgcn_s_sleep(ODELAY);
    STAMP(10);
    poll_a(rp, ooff);
    __builtin_amdgcn_s_sleep(PGAP);
    poll_b(rp, ooff);
    v4u um = take_c();
    if (__builtin_expect(!all_of(tag_ok(um, tag)), 0)) {
      n = 0;
      do {
        asm volatile("" ::: "memory");
        um = ws_ld(rw, moff);
      } while (!all_of(tag_ok(um, tag)) && ++n < kSpinGlb);
      STAMPV(25, n + 1);
    }
    const float ms = __uint_as_float(um.x), Mx = x32_max(x16_max(ms));
    const float w = __builtin_amdgcn_exp2f((ms - Mx) * 1.4426950408889634f);
    const float sc2 = w * __builtin_amdgcn_rcpf(x32_sum(x16_sum(w * __uint_as_float(um.z))));
    lds_spin_ge(F_LP, TH_WO);
    const v4u wf0 = tr_frag(L + RA + wb, 4096), wf1 = tr_frag(L + RA + wb + 8192, 4096);
    asm volatile("" ::"v"(sc2), "v"(wf0), "v"(wf1));
    const v2u uo = poll_end(rp, ooff, tag, n);
    STAMPV(22, 0x1000 - n);
    STAMP(11);
    const float o0 = x32_sum(x16_sum(sc2 * bflo(uo.x))), o1 = x32_sum(x16_sum(sc2 * bfhi(uo.x))),
                o2 = x32_sum(x16_sum(sc2 * bflo(uo.y))), o3 = x32_sum(x16_sum(sc2 * bfhi(uo.y)));
    if (lg == 0)
      *(LAS v2u*)(L + olw) = v2u{pk(o0, o1), pk(o2, o3)};
    asm volatile("s_waitcnt lgkmcnt(0)" ::: "memory");
    __builtin_amdgcn_s_barrier();
    asm volatile("" ::: "memory");
    f32x4 acc = mfma(wf0, *(LAS v4u*)(L + olr), f32x4{0.f, 0.f, 0.f, 0.f});
    acc = mfma(wf1, *(LAS v4u*)(L + olr + 512), acc);
    ws_st(rw, spw, v4u{__float_as_uint(lc < 8 ? acc[0] : acc[2]), tag, __float_as_uint(lc < 8 ? acc[1] : acc[3]), tag});
  }
  STAMP(12);
  {
    __builtin_amdgcn_s_sleep(SPDELAY);
    STAMP(13);
    poll_a(rp, soff);
    __builtin_amdgcn_s_sleep(PGAP);
    poll_b(rp, soff);
    const v2u us = poll_end(rp, soff, tag, n);
    STAMPV(23, 0x1000 - n);
    STAMP(14);
    float s0 = __uint_as_float(us.x), s1 = __uint_as_float(us.y);
    s0 += dpp<XOR8>(s0); s1 += dpp<XOR8>(s1);
    s0 = x32_sum(x16_sum(s0)); s1 = x32_sum(x16_sum(s1));
    if (lane < 8) { *(LAS float*)(L + frw) = s0; *(LAS float*)(L + frw + 32) = s1; }
    asm volatile("s_waitcnt lgkmcnt(0)" ::: "memory");
    __builtin_amdgcn_s_barrier();
    STAMP(15);
    STAMP_DUMP();
  }
}

__global__ void p_lead(unsigned char* ws) { ((u32*)(ws + (100 << 20)))[0] = now(); }
extern "C" void kernel_launch(void* const* d_in, const int* in_sizes, int n_in, void* d_out, int out_size, void* d_ws,
                              size_t ws_size, hipStream_t stream) {
  fused_decode_block<<<256, 640, 0, stream>>>((const u16*)d_in[0], (const u16*)d_in[1], (const u16*)d_in[2], (const u16*)d_in[3],
                                              (const u16*)d_in[4], (const u16*)d_in[6], (const u16*)d_in[7], (const u16*)d_in[8],
                                              (u16*)d_out, (unsigned char*)d_ws);
}
```

```cpp
#include <hip/hip_runtime.h>

typedef unsigned short u16;
typedef unsigned u32;
typedef unsigned long long u64;
typedef u32 v4u __attribute__((ext_vector_type(4)));
typedef u32 v2u __attribute__((ext_vector_type(2)));
typedef int s4i __attribute__((ext_vector_type(4)));
typedef float f32x4 __attribute__((ext_vector_type(4)));
typedef float f32x2 __attribute__((ext_vector_type(2)));
typedef short s16x4 __attribute__((ext_vector_type(4)));
typedef __bf16 bf16x8 __attribute__((ext_vector_type(8)));
typedef __bf16 bf16x2 __attribute__((ext_vector_type(2)));
#define LAS __attribute__((address_space(3)))
#define GAS __attribute__((address_space(1)))
typedef __amdgpu_buffer_rsrc_t rsrc_t;

#define LAG 3
#define P0 100
#define PACE 60
#define TQA 330
#define TQ1 60
#define TO 720
#define HGO 2
#define ODELAY 10
#define SPDELAY 13
#define PGAP 5

namespace {
constexpr int kD = 512, kSK = 4096, kW0 = 3072, kF = 1792;
constexpr int RK = 0, RV = 65536, RT = RV + 49152, RA = 131072, RD = 147456, RX = RD, RM = 155648;
constexpr int XN = RM, OL = RM, FR = RM + 1024, FG = RM + 2048, PL = RM + 2176;
constexpr int QL = RM + 4224, MLM = RM + 5376, MLL = RM + 5504, HL = RM + 5632, FL = RM + 6144, EL = FL + 64;
constexpr int F_LP = FL, F_XN = FL + 8, F_T = FL + 12, F_Q = FL + 16, F_P = FL + 20, F_X = FL + 24;
constexpr int LDS_BYTES = 163840;
constexpr int WS_E = 0, WS_QP = 4096, WS_HP = WS_QP + 2 * 8 * 4096 * 8, WS_OP = WS_HP + 2 * 2 * 8 * 896 * 8,
              WS_ML = WS_OP + 64 * 8 * 4 * 256, WS_SP = WS_ML + 8 * 8 * 4 * 64, WS_DP = WS_SP + 64 * 32 * 512,
              WS_END = WS_DP + 56 * 32 * 512;
constexpr int OOB = 0x7ffffff0;
constexpr int AUX_NT = 2, AUX_SC1 = 16, RSRC3 = 0x00020000;
constexpr u32 kSpinGlb = 1u << 13;
constexpr float kLog2e = 1.4426950408889634f;

__device__ __forceinline__ float bflo(u32 w) { return __builtin_bit_cast(float, w << 16); }
__device__ __forceinline__ float bfhi(u32 w) { return __builtin_bit_cast(float, w & 0xffff0000u); }
__device__ __forceinline__ u32 pk(float lo, float hi) {
  bf16x2 v = {(__bf16)lo, (__bf16)hi};
  return __builtin_bit_cast(u32, v);
}
__device__ __forceinline__ float rbf(float f) { return (float)(__bf16)f; }
__device__ __forceinline__ f32x4 mfma(v4u a, v4u b, f32x4 c) {
  return __builtin_amdgcn_mfma_f32_16x16x32_bf16(__builtin_bit_cast(bf16x8, a), __builtin_bit_cast(bf16x8, b), c, 0, 0, 0);
}
template <int CTRL> __device__ __forceinline__ float dpp(float x) {
  return __builtin_bit_cast(float, __builtin_amdgcn_mov_dpp(__builtin_bit_cast(int, x), CTRL, 0xf, 0xf, true));
}
constexpr int XOR1 = 0xB1, XOR2 = 0x4E, XOR7 = 0x141, XOR8 = 0x128;
__device__ __forceinline__ float x16_sum(float x) {
  auto s = __builtin_amdgcn_permlane16_swap(__float_as_uint(x), __float_as_uint(x), false, false);
  return __uint_as_float(s[0]) + __uint_as_float(s[1]);
}
__device__ __forceinline__ float x32_sum(float x) {
  auto s = __builtin_amdgcn_permlane32_swap(__float_as_uint(x), __float_as_uint(x), false, false);
  return __uint_as_float(s[0]) + __uint_as_float(s[1]);
}
__device__ __forceinline__ float vmax(float a, float b) { float r; asm("v_max_f32 %0, %1, %2" : "=v"(r) : "v"(a), "v"(b)); return r; }
__device__ __forceinline__ float vmax3(float a, float b, float c) { float r; asm("v_max3_f32 %0, %1, %2, %3" : "=v"(r) : "v"(a), "v"(b), "v"(c)); return r; }
__device__ __forceinline__ float row_max(float x) {
  asm("s_nop 1\n\tv_max_f32_dpp %0, %0, %0 quad_perm:[1,0,3,2] row_mask:0xf bank_mask:0xf\n\t"
      "s_nop 1\n\tv_max_f32_dpp %0, %0, %0 quad_perm:[2,3,0,1] row_mask:0xf bank_mask:0xf\n\t"
      "s_nop 1\n\tv_max_f32_dpp %0, %0, %0 row_half_mirror row_mask:0xf bank_mask:0xf\n\t"
      "s_nop 1\n\tv_max_f32_dpp %0, %0, %0 row_ror:8 row_mask:0xf bank_mask:0xf"
      : "+v"(x));
  return x;
}
__device__ __forceinline__ float x16_max(float x) {
  auto s = __builtin_amdgcn_permlane16_swap(__float_as_uint(x), __float_as_uint(x), false, false);
  return vmax(__uint_as_float(s[0]), __uint_as_float(s[1]));
}
__device__ __forceinline__ float x32_max(float x) {
  auto s = __builtin_amdgcn_permlane32_swap(__float_as_uint(x), __float_as_uint(x), false, false);
  return vmax(__uint_as_float(s[0]), __uint_as_float(s[1]));
}
__device__ __forceinline__ float x8_16_32_sum(float x) { x += dpp<XOR8>(x); return x32_sum(x16_sum(x)); }
__device__ __forceinline__ float wave_sum(float x) {
  x += dpp<XOR1>(x); x += dpp<XOR2>(x); x += dpp<XOR7>(x);
  return x8_16_32_sum(x);
}
__device__ __forceinline__ f32x4 fma4(float a, f32x4 b, f32x4 c) { return __builtin_elementwise_fma(f32x4{a, a, a, a}, b, c); }
__device__ __forceinline__ float silu(float x) { return x * __builtin_amdgcn_rcpf(1.f + __builtin_amdgcn_exp2f(x * -kLog2e)); }

__device__ __forceinline__ void lds_set(int addr, u32 val) { asm volatile("ds_write_b32 %0, %1" ::"v"(addr), "v"(val) : "memory"); }
__device__ __forceinline__ u32 lds_get(int addr) {
  u32 r;
  asm volatile("ds_read_b32 %0, %1\n\ts_waitcnt lgkmcnt(0)" : "=v"(r) : "v"(addr) : "memory");
  return __builtin_amdgcn_readfirstlane(r);
}
template <int SLEEP> __device__ __forceinline__ u32 lds_until(int addr, u32 want) {
  u32 v, r, cnt;
  asm volatile("s_mov_b32 %[cnt], 0x40000\n"
               ".Lspin_%=:\n\t"
               "ds_read_b32 %[r], %[a]\n\t"
               "s_waitcnt lgkmcnt(0)\n\t"
               "v_readfirstlane_b32 %[v], %[r]\n\t"
               "s_cmp_ge_u32 %[v], %[w]\n\t"
               "s_cbranch_scc1 .Lspin_done_%=\n\t"
               "s_sleep %[sl]\n\t"
               "s_sub_u32 %[cnt], %[cnt], 1\n\t"
               "s_cbranch_scc0 .Lspin_%=\n"
               ".Lspin_done_%=:"
               : [v] "=&s"(v), [r] "=&v"(r), [cnt] "=&s"(cnt)
               : [a] "v"(addr), [w] "s"(want), [sl] "n"(SLEEP)
               : "memory", "scc");
  return v;
}
__device__ __forceinline__ u32 lds_wait_ge(int addr, u32 want) { return lds_until<1>(addr, want); }
__device__ __forceinline__ u32 lds_spin_ge(int addr, u32 want) { return lds_until<0>(addr, want); }
template <int SLEEP> __device__ __forceinline__ void lds_until2(int addr, u32 want) {
  lds_until<SLEEP>(addr, want);
  lds_until<SLEEP>(addr + 4, want);
}
__device__ __forceinline__ u32 lds_spin_min2(int addr, u32 want) {
  u32 v, t, cnt;
  asm volatile("s_mov_b32 %[cnt], 0x40000\n"
               ".Lspin2_%=:\n\t"
               "ds_read_b64 v[164:165], %[a]\n\t"
               "s_waitcnt lgkmcnt(0)\n\t"
               "v_readfirstlane_b32 %[v], v164\n\t"
               "v_readfirstlane_b32 %[t], v165\n\t"
               "s_min_u32 %[v], %[v], %[t]\n\t"
               "s_cmp_ge_u32 %[v], %[w]\n\t"
               "s_cbranch_scc1 .Lspin2_done_%=\n\t"
               "s_sub_u32 %[cnt], %[cnt], 1\n\t"
               "s_cbranch_scc0 .Lspin2_%=\n"
               ".Lspin2_done_%=:"
               : [v] "=&s"(v), [t] "=&s"(t), [cnt] "=&s"(cnt)
               : [a] "v"(addr), [w] "s"(want)
               : "memory", "scc", "v164", "v165");
  return v;
}
__device__ __forceinline__ void lds_arrive(int addr) {
  asm volatile("s_mov_b64 exec, 1\n\tds_add_u32 %0, %1\n\ts_mov_b64 exec, -1" ::"v"(addr), "v"(1u) : "memory");
}
__device__ __forceinline__ v4u ws_ld(rsrc_t rs, int off) { return __builtin_amdgcn_raw_buffer_load_b128(rs, off, 0, AUX_SC1); }
__device__ __forceinline__ void ws_st(rsrc_t rs, int off, v4u v) { __builtin_amdgcn_raw_buffer_store_b128(v, rs, off, 0, AUX_SC1); }
__device__ __forceinline__ bool tag_ok(v4u u, u32 tag) { return u.y == tag && u.w == tag; }
__device__ __forceinline__ bool all_of(bool ok) { return __builtin_amdgcn_ballot_w64(!ok) == 0ull; }
__device__ __forceinline__ v4u tr_frag(LAS unsigned char* p, int half) {
  const v2u a = __builtin_bit_cast(v2u, __builtin_amdgcn_ds_read_tr16_b64_v4i16((LAS s16x4*)p));
  const v2u b = __builtin_bit_cast(v2u, __builtin_amdgcn_ds_read_tr16_b64_v4i16((LAS s16x4*)(p + half)));
  return v4u{a.x, a.y, b.x, b.y};
}
#define POLL_REGS "v156", "v157", "v158", "v159", "v160", "v161", "v162", "v163", "v164", "v165", "v166", "v167"
__device__ __forceinline__ void poll_a(s4i rs, int off) {
  asm volatile("buffer_load_dwordx4 v[156:159], %0, %1, 0 offen sc1" ::"v"(off), "s"(rs) : "memory", POLL_REGS);
}
__device__ __forceinline__ void poll_b(s4i rs, int off) {
  asm volatile("buffer_load_dwordx4 v[160:163], %0, %1, 0 offen sc1" ::"v"(off), "s"(rs) : "memory", POLL_REGS);
}
__device__ __forceinline__ void poll_c(s4i rs, int off) {
  asm volatile("buffer_load_dwordx4 v[164:167], %0, %1, 0 offen sc1" ::"v"(off), "s"(rs) : "memory", POLL_REGS);
}
__device__ __forceinline__ v4u take_c() {
  v4u u;
  asm volatile("s_waitcnt vmcnt(2)\n\tv_mov_b32 %0, v164\n\tv_mov_b32 %1, v165\n\tv_mov_b32 %2, v166\n\tv_mov_b32 %3, v167"
               : "=v"(u.x), "=v"(u.y), "=v"(u.z), "=v"(u.w)::"memory", POLL_REGS);
  return u;
}
__device__ __forceinline__ v2u poll_end(s4i rs, int off, u32 tag, u32& left) {
  u32 x, z, cnt;
  int ad;
  u64 ok, t;
#define POLL_STEP(r0, r1, r2, r3)                                 \
  "s_waitcnt vmcnt(1)\n\t"                                        \
  "v_cmp_eq_u32_e32 vcc, %[tag], v" #r1 "\n\t"                    \
  "v_cmp_eq_u32_e64 %[t], %[tag], v" #r3 "\n\t"                   \
  "s_and_b64 vcc, vcc, %[t]\n\t"                                  \
  "v_cndmask_b32_e32 %[x], %[x], v" #r0 ", vcc\n\t"               \
  "v_cndmask_b32_e32 %[z], %[z], v" #r2 ", vcc\n\t"               \
  "s_or_b64 %[ok], %[ok], vcc\n\t"                                \
  "s_cmp_eq_u64 %[ok], -1\n\t"                                    \
  "s_cbranch_scc1 .Lpoll_done_%=\n\t"                             \
  "v_cndmask_b32_e64 %[ad], %[off], %[oob], %[ok]\n\t"            \
  "buffer_load_dwordx4 v[" #r0 ":" #r3 "], %[ad], %[rs], 0 offen sc1\n\t"
  asm volatile("s_mov_b64 %[ok], 0\n\t"
               "s_movk_i32 %[cnt], 0x1000\n"
               ".Lpoll_loop_%=:\n\t" POLL_STEP(156, 157, 158, 159) POLL_STEP(160, 161, 162, 163)
               "s_sub_u32 %[cnt], %[cnt], 1\n\t"
               "s_cmp_lg_u32 %[cnt], 0\n\t"
               "s_cbranch_scc1 .Lpoll_loop_%=\n"
               ".Lpoll_done_%=:\n\t"
               "s_nop 0"
               : [x] "=&v"(x), [z] "=&v"(z), [ad] "=&v"(ad), [ok] "=&s"(ok), [t] "=&s"(t), [cnt] "=&s"(cnt)
               : [off] "v"(off), [oob] "v"(OOB), [rs] "s"(rs), [tag] "s"(tag)
               : "memory", "vcc", "scc", POLL_REGS);
  left = cnt;
  return v2u{x, z};
}
__device__ __forceinline__ u32 next_tag(u32 e) { return e + 1u == 0u ? 1u : e + 1u; }
__device__ __forceinline__ u32 now() { return (u32)__builtin_amdgcn_s_memrealtime(); }
__device__ __forceinline__ void wait_until(u32 t0, u32 dt) {
  while (now() - t0 < dt) __builtin_amdgcn_s_sleep(1);
}
}

#define PAD(k) asm volatile("s_branch .Lpad_skip" #k "\n.Lpad" #k ":\n\ts_setpc_b64 s[94:95]\n.Lpad_skip" #k ":")
#define PADP(k) asm volatile("s_branch .Lpad_skip" #k "\n\t.p2align 12\n.Lpad" #k ":\n\ts_setpc_b64 s[94:95]\n.Lpad_skip" #k ":")
#define PAD_SEL(w, k) "s_cmp_eq_u32 %[w], " #w "\n\ts_cselect_b32 s92, .Lpad" #k "-.Lpad_base%=, s92\n\t"
#define PAD_HEAD "s_getpc_b64 s[90:91]\n.Lpad_base%=:\n\t"
#define PAD_GO "s_ashr_i32 s93, s92, 31\n\ts_add_u32 s92, s90, s92\n\ts_addc_u32 s93, s91, s93\n\ts_swappc_b64 s[94:95], s[92:93]\n\t"

typedef u32 s8u __attribute__((ext_vector_type(8)));
typedef u32 s4u __attribute__((ext_vector_type(4)));
template <class T> __device__ __forceinline__ T* mkptr(u32 lo, u32 hi) { return (T*)(((u64)hi << 32) | lo); }

__global__ void __launch_bounds__(704, 3)
fused_decode_block(const u16* x_, const u16* kc_, const u16* vc_, const u16* wq_, const u16* wo_, const u16* wg_, const u16* wu_,
                   const u16* wd_, u16* out_, unsigned char* ws_) {
  __shared__ __attribute__((aligned(16))) unsigned char lds[LDS_BYTES];
  LAS unsigned char* L = (LAS unsigned char*)lds;
  const int tid = threadIdx.x, lane = tid & 63, wave = __builtin_amdgcn_readfirstlane(tid >> 6);
  s8u ka, kb;
  s4u kd;
  int c = blockIdx.x;
  asm volatile("ds_write_b32 %[fa], %[fz]\n\t"
               "s_waitcnt lgkmcnt(0)\n\t"
               "s_load_dwordx8 %[a], %[p], 0x0\n\ts_load_dwordx8 %[b], %[p], 0x20\n\ts_load_dwordx4 %[d], %[p], 0x40\n\t"
               "s_barrier\n\t"
               "s_sub_u32 s92, %[w], 2\n\ts_cmp_gt_u32 s92, 4\n\ts_cbranch_scc1 .Lpad_none\n\t" PAD_HEAD PAD_SEL(2, 0) PAD_SEL(3, 1) PAD_SEL(4, 5)
               PAD_SEL(5, 2) PAD_SEL(6, 3) PAD_GO ".Lpad_none:\n\t"
               : [a] "=&s"(ka), [b] "=&s"(kb), [d] "=&s"(kd), [c] "+s"(c)
               : [p] "s"(__builtin_amdgcn_kernarg_segment_ptr()), [w] "s"(wave), [fa] "v"(FL + (lane & 7) * 4), [fz] "v"(0u)
               : "s90", "s91", "s92", "s93", "s94", "s95", "scc", "memory");
  const int g8 = c & 7, r = c >> 3;
  const int u1 = c >> 1;
  const bool is_q = !(c & 1), has_t = is_q || u1 < 112;
  int kh = is_q ? u1 >> 6 : u1 >= 56;
  const int t56 = u1 - 56 * kh, mat = t56 >= 28;
  int ng = is_q ? u1 & 63 : t56 - 28 * mat;
  int fg = (r >> 2) * 8 + g8;
  const int cg = r & 3;
  const bool has_dn = fg < 56;
  const int dt = r >> 2;
  const int ab = r >> 2, as = r & 3;
  const bool is_fin = c < 128;
  const int fb = (c >> 5) & 3, fc = c & 31;
  asm volatile("s_waitcnt lgkmcnt(0)" : "+s"(ka), "+s"(kb), "+s"(kd), "+s"(kh), "+s"(ng), "+s"(fg));
  const u16 *x = mkptr<const u16>(ka[0], ka[1]), *kc = mkptr<const u16>(ka[2], ka[3]), *vc = mkptr<const u16>(ka[4], ka[5]),
            *wq = mkptr<const u16>(ka[6], ka[7]), *wo = mkptr<const u16>(kb[0], kb[1]), *wg = mkptr<const u16>(kb[2], kb[3]),
            *wu = mkptr<const u16>(kb[4], kb[5]), *wd = mkptr<const u16>(kb[6], kb[7]);
  GAS u16* out = (GAS u16*)mkptr<u16>(kd[0], kd[1]);
  unsigned char* ws = mkptr<unsigned char>(kd[2], kd[3]);
  const rsrc_t rw = __builtin_amdgcn_make_buffer_rsrc((void*)ws, 0, WS_END, RSRC3);
  const s4i rp = {(int)(u32)(u64)ws, (int)((u32)((u64)ws >> 32) & 0xffffu), WS_END, RSRC3};
  const u32 TH_K = has_t ? 3 : 1, TH_V = TH_K + 4, TH_DN = TH_V + 5, TH_WO = TH_V + 6;

  if (__builtin_expect(wave < 2, 1)) {
    const int r8 = lane >> 3, r4 = lane >> 4;
    const int sw128 = ((lane & 7) ^ ((r4 & 3) << 1)) * 16;
    const int swV = ((lane & 15) ^ (r4 << 1)) * 16;
    const int swK = ((lane & 15) ^ (r4 >> 1)) * 16;
    const int flp = F_LP + wave * 4;
    u32 cnt;
    const u32 t0 = now();
    u32 due = P0 + wave * (PACE / 2);
    __builtin_amdgcn_s_setprio(1);
#define GLDS(rs, ldsoff, voff, soff) __builtin_amdgcn_raw_ptr_buffer_load_lds(rs, (LAS void*)(L + (ldsoff)), 16, voff, soff, 0, AUX_NT)
#define LANDED(N, U) do { asm volatile("s_waitcnt vmcnt(%0)" ::"n"(N) : "memory"); lds_set(flp, U); } while (0)
#define GROUP_DONE() do { asm volatile("s_waitcnt vmcnt(%0)" ::"n"(LAG * 8) : "memory"); ++cnt; if (cnt > LAG) lds_set(flp, cnt + 1 - LAG); } while (0)
    {
      const rsrc_t rs = __builtin_amdgcn_make_buffer_rsrc((void*)x, 0, 8192, RSRC3);
#pragma unroll
      for (int i = 0; i < 4; ++i)
        __builtin_amdgcn_raw_ptr_buffer_load_lds(rs, (LAS void*)(L + RX + wave * 4096 + i * 1024), 16, lane * 16, wave * 4096 + i * 1024, 0, 0);
      __builtin_amdgcn_raw_ptr_buffer_load_lds(rw, (LAS void*)(L + EL + wave * 256), 4, lane * 4, WS_E, 0, AUX_SC1);
    }
    if (has_t) {
      const int pitch = is_q ? 8192 : kF * 2;
      const rsrc_t rs = __builtin_amdgcn_make_buffer_rsrc((void*)(is_q ? wq : mat ? wu : wg), 0, 512 * pitch, RSRC3);
      const int vo = r8 * pitch + sw128;
      int so = (kh * 256 + wave * 128) * pitch + ng * 128;
#pragma unroll 1
      for (int n = 0; n < 2; ++n) {
#pragma unroll
        for (int i = 0; i < 8; ++i) { GLDS(rs, RT + wave * 16384 + n * 8192 + i * 1024, vo, so); so += 8 * pitch; }
      }
      asm volatile("s_waitcnt vmcnt(16)" ::: "memory");
      cnt = 2;
    } else {
      asm volatile("s_waitcnt vmcnt(0)" ::: "memory");
      cnt = 0;
    }
    lds_set(flp, 1u);
    const int rot = ((c * 37 + r * 11) & 63) * 1024 + wave * 8192;
    const size_t kvoff = ((size_t)(ab * 8 + g8) * kSK + kW0 + as * 256) * 128;
    {
      const rsrc_t rs = __builtin_amdgcn_make_buffer_rsrc((void*)(kc + kvoff), 0, 65536, RSRC3);
      const int vo = r4 * 256;
#pragma unroll 1
      for (int n = 0; n < 4; ++n) {
        wait_until(t0, due);
        due += PACE;
#pragma unroll
        for (int i = 0; i < 8; ++i)
          GLDS(rs, RK + wave * 8192 + n * 16384 + i * 1024, vo + (swK ^ (i * 32)), (rot + n * 16384 + i * 1024) & 65535);
        if (n == 0) { LANDED(8, cnt + 1); ++cnt; } else if (n < LAG - 1 || n == 1) ++cnt; else GROUP_DONE();
      }
    }
    u32 xfin;
    lds_until2<1>(F_LP, 1);
    asm volatile("ds_read_u16 %0, %1" : "=v"(xfin) : "v"(RX + ((2 * fb + ((lane >> 4) & 1)) * kD + fc * 16 + (lane & 15)) * 2) : "memory");
    const u32 tag = next_tag(lds_get(EL));
    asm volatile("" : "+v"(xfin));
    if (wave == 0) lds_set(F_X, 1u);
    {
      const rsrc_t rs = __builtin_amdgcn_make_buffer_rsrc((void*)(vc + kvoff), 0, 65536, RSRC3);
      const int vo = r4 * 256;
#pragma unroll 1
      for (int n = 0; n < 4; ++n) {
        wait_until(t0, due);
        due += PACE;
        if (n == 3) lds_wait_ge(F_T, 2);
#pragma unroll
        for (int i = 0; i < 8; ++i)
          GLDS(rs, RV + wave * 8192 + n * 16384 + i * 1024, vo + (swV ^ ((i & 1) * 128)), (rot + n * 16384 + i * 1024) & 65535);
        GROUP_DONE();
      }
    }
    lds_wait_ge(F_X, 1);
    {
      const rsrc_t rs = __builtin_amdgcn_make_buffer_rsrc((void*)wd, 0, has_dn ? kF * 512 * 2 : 0, RSRC3);
      const int vo = r4 * 1024, so = fg * 32 * 1024 + cg * 256 + wave * 16384;
#pragma unroll
      for (int i = 0; i < 4; ++i) GLDS(rs, RD + wave * 4096 + i * 1024, vo + (swV ^ ((i & 1) * 128)), so + i * 4096);
    }
    LANDED(20, cnt - 1);
    LANDED(12, cnt);
    LANDED(4, cnt + 1);
    {
      const rsrc_t rs = __builtin_amdgcn_make_buffer_rsrc((void*)wo, 0, 4096 * 512 * 2, RSRC3);
      const int vo = r4 * 1024, so = (g8 * 512 + wave * 256 + dt * 16) * 1024 + cg * 256;
#pragma unroll
      for (int i = 0; i < 8; ++i)
        GLDS(rs, RA + wave * 8192 + i * 1024, vo + (swV ^ ((i & 1) * 128)), so + ((i >> 2) * 128 + (i & 3) * 4) * 1024);
    }
    LANDED(8, cnt + 2);
    LANDED(0, cnt + 3);
    if (wave == 1 || !is_fin) return;
    __builtin_amdgcn_s_barrier();
    float gate;
    {
      const int doff = WS_DP + ((lane >> 3) * 32 + fc) * 512 + fb * 128 + (lane & 7) * 16;
      float f[4];
      for (u32 n = 0; n < kSpinGlb; ++n) {
        asm volatile("" ::: "memory");
        v4u u[7];
#pragma unroll
        for (int i = 0; i < 7; ++i) u[i] = ws_ld(rw, doff + i * (8 * 32 * 512));
        bool ok = true;
        f[0] = f[1] = f[2] = f[3] = 0.f;
#pragma unroll
        for (int i = 0; i < 7; ++i) {
          ok = ok && tag_ok(u[i], tag);
          f[0] += bflo(u[i].x); f[1] += bfhi(u[i].x); f[2] += bflo(u[i].z); f[3] += bfhi(u[i].z);
        }
        if (all_of(ok)) break;
      }
#pragma unroll
      for (int i = 0; i < 4; ++i) f[i] = x8_16_32_sum(f[i]);
      if (lane < 8) *(LAS f32x4*)(L + FG + lane * 16) = f32x4{f[0], f[1], f[2], f[3]};
      gate = rbf(silu(rbf(*(LAS float*)(L + FG + (lane & 31) * 4))));
      asm volatile("" ::"v"(gate));
    }
    PADP(0);
    __builtin_amdgcn_s_barrier();
    asm volatile("" ::: "memory");
    const f32x4 a0 = *(LAS f32x4*)(L + FR + (lane & 31) * 32), a1 = *(LAS f32x4*)(L + FR + (lane & 31) * 32 + 16);
    const float sv = rbf(((a0[0] + a0[1]) + (a0[2] + a0[3])) + ((a1[0] + a1[1]) + (a1[2] + a1[3])));
    const float o = bflo(xfin) + rbf(sv * gate);
    if (lane < 32) out[(2 * fb + (lane >> 4)) * kD + fc * 16 + (lane & 15)] = (u16)pk(o, o);
    if (c == 0 && lane == 0) __builtin_amdgcn_raw_buffer_store_b32(tag, rw, WS_E, 0, AUX_SC1);
    return;
  }
  const u32 t0 = now();
  const int cw = wave - 2, lc = lane & 15, lg = lane >> 4;
  const int tq = lc >> 2, tp = lc & 3, row0 = 4 * lg + tq;
  int opw = WS_OP + (((g8 * 8 + cw) * 8 + ab) * 4 + as) * 256 + lc * 64 + lg * 16;
  int spw = WS_SP + ((g8 * 8 + dt) * 32 + cg * 8 + cw) * 512 + lc * 64 + lg * 16;

  if (wave == 10) {
    if (!has_dn) return;
    lds_until<4>(F_LP, TH_V + HGO);
    const u32 tag = next_tag(lds_get(EL));
    const int hoff = WS_HP + ((lane >> 3) * 896 + fg * 16 + (lane & 7) * 2) * 8;
    float g[4], u[4];
    for (u32 n = 0; n < kSpinGlb; ++n) {
      asm volatile("" ::: "memory");
      v4u t[4];
      __builtin_amdgcn_s_setprio(3);
#pragma unroll
      for (int i = 0; i < 4; ++i) t[i] = ws_ld(rw, hoff + i * (8 * 896 * 8));
      __builtin_amdgcn_s_setprio(0);
      g[0] = bflo(t[0].x) + bflo(t[2].x); g[1] = bfhi(t[0].x) + bfhi(t[2].x); g[2] = bflo(t[0].z) + bflo(t[2].z); g[3] = bfhi(t[0].z) + bfhi(t[2].z);
      u[0] = bflo(t[1].x) + bflo(t[3].x); u[1] = bfhi(t[1].x) + bfhi(t[3].x); u[2] = bflo(t[1].z) + bflo(t[3].z); u[3] = bfhi(t[1].z) + bfhi(t[3].z);
      if (all_of(tag_ok(t[0], tag) && tag_ok(t[1], tag) && tag_ok(t[2], tag) && tag_ok(t[3], tag))) break;
    }
#pragma unroll
    for (int i = 0; i < 4; ++i) g[i] = rbf(silu(rbf(g[i]))) * rbf(u[i]);
    *(LAS v2u*)(L + HL + ((lane & 3) * 8 + (lane >> 3)) * 16 + ((lane >> 2) & 1) * 8) = v2u{pk(g[0], g[1]), pk(g[2], g[3])};
    const v4u hf = *(LAS v4u*)(L + HL + (lg * 8 + (lc & 7)) * 16);
    const int dpw = lc < 8 ? WS_DP + (fg * 32 + cg * 8) * 512 + lc * 64 + lg * 16 : OOB;
    lds_until2<1>(F_LP, TH_DN);
#pragma unroll
    for (int i = 0; i < 8; ++i) {
      const f32x4 acc = mfma(tr_frag(L + RD + row0 * 256 + (((2 * i + (tp >> 1)) ^ ((row0 & 7) << 1)) * 16) + (tp & 1) * 8, 4096), hf,
                             f32x4{0.f, 0.f, 0.f, 0.f});
      __builtin_amdgcn_raw_buffer_store_b128(v4u{pk(acc[0], acc[1]), tag, pk(acc[2], acc[3]), tag}, rw, dpw, i * 512, AUX_SC1);
    }
    __builtin_amdgcn_s_barrier();
    if (is_fin) __builtin_amdgcn_s_barrier();
    return;
  }

  lds_spin_min2(F_LP, 1);
  const u32 tag = next_tag(lds_get(EL));
  {
    const v4u xr = *(LAS v4u*)(L + RX + cw * 1024 + lane * 16);
    float v[8];
#pragma unroll
    for (int i = 0; i < 4; ++i) { v[2 * i] = bflo(xr[i]); v[2 * i + 1] = bfhi(xr[i]); }
    const float x0 = __builtin_bit_cast(float, __builtin_amdgcn_readfirstlane(xr[0] << 16));
    float s0 = 0.f, q0 = 0.f;
#pragma unroll
    for (int i = 0; i < 8; ++i) { const float d = v[i] - x0; s0 += d; q0 = fmaf(d, d, q0); }
    s0 = wave_sum(s0) * (1.f / kD);
    q0 = wave_sum(q0) * (1.f / kD);
    const float mb = rbf(x0 + s0), vb = rbf(fmaxf(fmaf(-s0, s0, q0), 0.f));
    const float rden = __builtin_amdgcn_rcpf(rbf(__builtin_amdgcn_sqrtf(rbf(vb + 1e-5f))));
#pragma unroll
    for (int i = 0; i < 8; ++i) v[i] = rbf(v[i] - mb) * rden;
    if ((lane >> 5) == kh) {
      const int e = lane & 31, o = e & 3;
      LAS v2u* p = (LAS v2u*)(L + XN + (((e >> 2) * 4 + (o & 1) * 2) * 8 + cw) * 16 + (o >> 1) * 8);
      p[0] = v2u{pk(v[0], v[1]), pk(v[2], v[3])};
      p[16] = v2u{pk(v[4], v[5]), pk(v[6], v[7])};
    }
    lds_arrive(F_XN);
  }
  PAD(1);
  if (cw < 2) {
    lds_spin_ge(F_XN, 8);
    if (has_t) {
      v4u xf[8];
#pragma unroll
      for (int ks = 0; ks < 8; ++ks) xf[ks] = *(LAS v4u*)(L + XN + ((ks * 4 + lg) * 8 + (lc & 7)) * 16);
      int off = is_q ? WS_QP + ((kh * 8 + (lc & 7)) * 4096 + ng * 64 + cw * 32 + lg * 4 + (lc >> 3) * 2) * 8
                     : WS_HP + (((kh * 2 + mat) * 8 + (lc & 7)) * 896 + ng * 32 + cw * 16 + (lc >> 3) * 8 + lg * 2) * 8;
      int base = RT + row0 * 128 + (((4 * cw + (tp >> 1)) ^ (((row0 >> 1) & 3) << 1)) * 16) + (tp & 1) * 8;
      asm volatile("" : "+v"(off), "+v"(base));
      lds_spin_min2(F_LP, 3);
      __builtin_amdgcn_s_setprio(3);
      f32x4 a0 = {0.f, 0.f, 0.f, 0.f}, a1 = {0.f, 0.f, 0.f, 0.f};
#pragma unroll
      for (int ks = 0; ks < 8; ++ks) {
        a0 = mfma(tr_frag(L + base + ks * 4096, 2048), xf[ks], a0);
        a1 = mfma(tr_frag(L + (base ^ 32) + ks * 4096, 2048), xf[ks], a1);
      }
      if (is_q) {
        ws_st(rw, off, v4u{__float_as_uint(lc < 8 ? a0[0] : a0[2]), tag, __float_as_uint(lc < 8 ? a0[1] : a0[3]), tag});
        ws_st(rw, off + 128, v4u{__float_as_uint(lc < 8 ? a1[0] : a1[2]), tag, __float_as_uint(lc < 8 ? a1[1] : a1[3]), tag});
      } else {
        if (lc >= 8) a0 = a1;
        ws_st(rw, off, v4u{pk(a0[0], a0[1]), tag, pk(a0[2], a0[3]), tag});
      }
    }
    __builtin_amdgcn_s_setprio(0);
    lds_arrive(F_T);
  }
  PAD(5);
  __builtin_amdgcn_sched_barrier(0);
  u32 n, lp, got = 0;
#define READ_V(FROM)                                                                                              \
  _Pragma("unroll") for (int i_ = (FROM); i_ < 4; ++i_) if (got <= TH_V + i_ && lp > TH_V + i_) {                \
    vf[2 * i_] = tr_frag(L + vrd + i_ * 16384, 4096);                                                             \
    vf[2 * i_ + 1] = tr_frag(L + vrd + i_ * 16384 + 8192, 4096);                                                  \
  }                                                                                                               \
  got = lp
  v4u kf[8], vf[8];
  const int wb = row0 * 256 + (((2 * cw + (tp >> 1)) ^ ((row0 & 7) << 1)) * 16) + (tp & 1) * 8;
  int ooff = WS_OP + (((g8 * 8 + dt) * 8 + cw) * 4 + lg) * 256 + lc * 16;
  int moff = WS_ML + ((cw * 8 + g8) * 4 + lg) * 64 + (lc >> 2) * 16;
  int soff = WS_SP + ((cw * 8 + (lane >> 3)) * 32 + fc) * 512 + fb * 128 + (lane & 7) * 16;
  int olw = OL + (((lc >> 3) * 4 + (lc & 3)) * 8 + cw) * 16 + ((lc >> 2) & 1) * 8, olr = OL + (lg * 8 + (lc & 7)) * 16;
  int frw = FR + (lane & 7) * 128 + cw * 4;
  int plr = PL + (lg * 4 + tp) * 16, mlr = MLM + tp * 32, mlg = WS_ML + ((ab * 8 + g8) * 4 + as) * 64 + lane * 16, vrd = RV + wb;
  if (lc >= 4) opw = OOB;
  if (lc >= 8) spw = OOB;
  const int kp = (tp >> 1) * 8 + tq * 2 + (tp & 1);
  int qlw = QL + (cw >> 1) * 272 + (cw & 1) * 128 + (lg >> 1) * 64 + lc * 4, qlr = QL + tq * 272 + lg * 16;
  int plw = PL + ((cw * 4 + tq) * 4 + lg) * 16 + tp * 4, mlw = MLM + (lg * 8 + cw) * 4;
  {
    int qoff = WS_QP + (((lg & 1) * 8 + ab) * 4096 + g8 * 512 + cw * 64 + (lg >> 1) * 32 + lc * 2) * 8;
    int krd = RK + (cw * 32 + 2 * kp) * 256 + ((lg ^ kp) * 16);
    asm volatile("" : "+v"(qlw), "+v"(qlr), "+v"(plw), "+v"(mlw), "+v"(plr), "+v"(mlr), "+v"(mlg), "+v"(vrd));
    asm volatile("" : "+v"(ooff), "+v"(moff), "+v"(soff), "+v"(spw), "+v"(opw), "+v"(olw), "+v"(olr), "+v"(frw));
    asm volatile("" : "+v"(qoff), "+v"(krd));
    PAD(2);
    lds_wait_ge(F_T, 2);
    wait_until(now(), TQ1);
    wait_until(t0, TQA);
    __builtin_amdgcn_s_setprio(3);
    const v4u u4 = ws_ld(rw, qoff);
    ws_st(rw, opw, v4u{0u, 0u, 0u, 0u});
    __builtin_amdgcn_s_setprio(0);
    __builtin_amdgcn_sched_barrier(0);
    lds_wait_ge(F_LP + (cw & 1) * 4, TH_K + (cw >> 1) + 1);
#pragma unroll
    for (int i = 0; i < 8; ++i)
      kf[i] = *(LAS v4u*)(L + ((krd ^ ((i & 3) * 64)) + (i >> 2) * 256));
    asm volatile("" : "+v"(kf[0]), "+v"(kf[1]), "+v"(kf[2]), "+v"(kf[3]), "+v"(kf[4]), "+v"(kf[5]), "+v"(kf[6]), "+v"(kf[7]));
    v2u uq = {u4.x, u4.z};
    if (__builtin_expect(!all_of(tag_ok(u4, tag)), 0)) {
      __builtin_amdgcn_s_setprio(3);
      poll_a(rp, qoff);
      __builtin_amdgcn_s_sleep(4);
      poll_b(rp, qoff);
      uq = poll_end(rp, qoff, tag, n);
    }
    __builtin_amdgcn_s_setprio(2);
    const float q0 = x16_sum(__uint_as_float(uq.x)), q1 = x16_sum(__uint_as_float(uq.y));
    *(LAS u32*)(L + qlw) = pk(q0, q1);
    lds_arrive(F_Q);
  }
  {
    lp = lds_spin_min2(F_LP, 0);
    READ_V(0);
    PADP(3);
    lds_spin_ge(F_Q, 8);
    v4u qf[4];
#pragma unroll
    for (int ks = 0; ks < 4; ++ks) qf[ks] = *(LAS v4u*)(L + qlr + ks * 64);
    asm volatile("" : "+v"(qf[0]), "+v"(qf[1]), "+v"(qf[2]), "+v"(qf[3]));
    f32x4 a0 = {0.f, 0.f, 0.f, 0.f}, a1 = {0.f, 0.f, 0.f, 0.f};
#pragma unroll
    for (int ks = 0; ks < 4; ++ks) { a0 = mfma(qf[ks], kf[ks], a0); a1 = mfma(qf[ks], kf[4 + ks], a1); }
    u32 r0 = pk(a0[0], a1[0]);
    asm volatile("" : "+v"(r0));
    const f32x2 t2 = f32x2{bflo(r0), bfhi(r0)} * 0.08838834764831845f;
    u32 r1 = pk(t2[0], t2[1]);
    asm volatile("" : "+v"(r1));
    const float s0 = bflo(r1), s1 = bfhi(r1);
    const float m = row_max(vmax3(s0, s1, -3.0e38f));
    const float p0 = __builtin_amdgcn_exp2f((s0 - m) * kLog2e), p1 = __builtin_amdgcn_exp2f((s1 - m) * kLog2e);
    *(LAS u32*)(L + plw) = pk(p0, p1);
    float l = p0 + p1;
    l += dpp<XOR1>(l); l += dpp<XOR2>(l); l += dpp<XOR7>(l); l += dpp<XOR8>(l);
    if (lc == 0) { *(LAS float*)(L + mlw) = m; *(LAS float*)(L + mlw + (MLL - MLM)) = l; }
    lds_arrive(F_P);
  }
  lds_spin_ge(F_P, 8);
  {
    lp = lds_spin_min2(F_LP, 0);
    READ_V(0);
    const f32x4 m0 = *(LAS f32x4*)(L + mlr), m1 = *(LAS f32x4*)(L + mlr + 16);
    v4u pf[8];
#pragma unroll
    for (int i = 0; i < 8; ++i) pf[i] = *(LAS v4u*)(L + plr + i * 256);
    __builtin_amdgcn_sched_barrier(0);
    const float M = vmax3(vmax3(m0[0], m0[1], m0[2]), vmax3(m0[3], m1[0], m1[1]), vmax(m1[2], m1[3]));
    const f32x4 e0 = (m0 - M) * kLog2e, e1 = (m1 - M) * kLog2e;
    float f[8];
#pragma unroll
    for (int i = 0; i < 4; ++i) { f[i] = __builtin_amdgcn_exp2f(e0[i]); f[4 + i] = __builtin_amdgcn_exp2f(e1[i]); }
    auto publish_ml = [&]() {
      if (cw == 0 && lane < 4) {
        const f32x4 l0 = *(LAS f32x4*)(L + mlr + (MLL - MLM)), l1 = *(LAS f32x4*)(L + mlr + (MLL - MLM) + 16);
        const float l = (fmaf(l0[0], f[0], l0[1] * f[1]) + fmaf(l0[2], f[2], l0[3] * f[3])) + (fmaf(l1[0], f[4], l1[1] * f[5]) + fmaf(l1[2], f[6], l1[3] * f[7]));
        ws_st(rw, mlg, v4u{__float_as_uint(M), tag, __float_as_uint(l), tag});
      }
    };
    const f32x4 z = {0.f, 0.f, 0.f, 0.f};
    f32x4 acc = z;
    publish_ml();
    if (lp >= TH_V + 4) {
      f32x4 d[8];
#pragma unroll
      for (int i = 0; i < 8; ++i) d[i] = mfma(vf[i], pf[i], z);
      __builtin_amdgcn_sched_barrier(0);
#pragma unroll
      for (int i = 0; i < 8; ++i) acc = fma4(f[i], d[i], acc);
    } else {
      __builtin_amdgcn_s_setprio(0);
#pragma unroll
      for (int i = 0; i < 4; ++i) {
        if (lp <= TH_V + i) {
          lp = lds_spin_min2(F_LP, TH_V + i + 1);
          READ_V(i);
        }
        const f32x4 d0 = mfma(vf[2 * i], pf[2 * i], z), d1 = mfma(vf[2 * i + 1], pf[2 * i + 1], z);
        acc = fma4(f[2 * i + 1], d1, fma4(f[2 * i], d0, acc));
      }
    }
    __builtin_amdgcn_s_setprio(3);
    ws_st(rw, opw, v4u{pk(acc[0], acc[1]), tag, pk(acc[2], acc[3]), tag});
    ws_st(rw, spw, v4u{0u, 0u, 0u, 0u});
  }
  {
    __builtin_amdgcn_s_sleep(ODELAY);
    wait_until(t0, TO);
    poll_c(rp, moff);
    poll_a(rp, ooff);
    __builtin_amdgcn_s_sleep(PGAP);
    poll_b(rp, ooff);
    auto scale_of = [&](v4u um) {
      const float ms = __uint_as_float(um.x), w = __builtin_amdgcn_exp2f((ms - x32_max(x16_max(ms))) * kLog2e);
      return w * __builtin_amdgcn_rcpf(x32_sum(x16_sum(w * __uint_as_float(um.z))));
    };
    v4u um = take_c();
    const bool m_ok = all_of(tag_ok(um, tag));
    float sc2 = scale_of(um);
    lds_spin_min2(F_LP, TH_WO);
    const v4u wf0 = tr_frag(L + vrd + (RA - RV), 4096), wf1 = tr_frag(L + vrd + (RA - RV) + 8192, 4096);
    asm volatile("" ::"v"(sc2), "v"(wf0), "v"(wf1));
    const v2u uo = poll_end(rp, ooff, tag, n);
    if (__builtin_expect(!m_ok, 0)) {
      u32 k = 0;
      do {
        asm volatile("" ::: "memory");
        um = ws_ld(rw, moff);
      } while (!all_of(tag_ok(um, tag)) && ++k < kSpinGlb);
      sc2 = scale_of(um);
    }
    const float o0 = x32_sum(x16_sum(sc2 * bflo(uo.x))), o1 = x32_sum(x16_sum(sc2 * bfhi(uo.x))),
                o2 = x32_sum(x16_sum(sc2 * bflo(uo.y))), o3 = x32_sum(x16_sum(sc2 * bfhi(uo.y)));
    if (lg == 0)
      *(LAS v2u*)(L + olw) = v2u{pk(o0, o1), pk(o2, o3)};
    asm volatile("s_waitcnt lgkmcnt(0)" ::: "memory");
    __builtin_amdgcn_s_barrier();
    asm volatile("" ::: "memory");
    f32x4 acc = mfma(wf0, *(LAS v4u*)(L + olr), f32x4{0.f, 0.f, 0.f, 0.f});
    acc = mfma(wf1, *(LAS v4u*)(L + olr + 512), acc);
    ws_st(rw, spw, v4u{pk(acc[0], acc[1]), tag, pk(acc[2], acc[3]), tag});
  }
  if (!is_fin) return;
  {
    __builtin_amdgcn_s_sleep(SPDELAY);
    poll_a(rp, soff);
    __builtin_amdgcn_s_sleep(PGAP);
    poll_b(rp, soff);
    const v2u us = poll_end(rp, soff, tag, n);
    const float s0 = x8_16_32_sum(bflo(us.x)), s1 = x8_16_32_sum(bfhi(us.x)), s2 = x8_16_32_sum(bflo(us.y)), s3 = x8_16_32_sum(bfhi(us.y));
    if (lane < 8) {
      *(LAS float*)(L + frw) = s0; *(LAS float*)(L + frw + 32) = s1; *(LAS float*)(L + frw + 64) = s2; *(LAS float*)(L + frw + 96) = s3;
    }
    asm volatile("s_waitcnt lgkmcnt(0)" ::: "memory");
    __builtin_amdgcn_s_barrier();
  }
}

extern "C" void kernel_launch(void* const* d_in, const int* in_sizes, int n_in, void* d_out, int out_size, void* d_ws,
                              size_t ws_size, hipStream_t stream) {
  fused_decode_block<<<256, 704, 0, stream>>>((const u16*)d_in[0], (const u16*)d_in[1], (const u16*)d_in[2], (const u16*)d_in[3],
                                              (const u16*)d_in[4], (const u16*)d_in[6], (const u16*)d_in[7], (const u16*)d_in[8],
                                              (u16*)d_out, (unsigned char*)d_ws);
}
```
